```python
import torch

def _softmax(x, axis=-1):
    return torch.softmax(x, dim=axis)
def _gelu(x):
    return torch.nn.functional.gelu(x, approximate="tanh")
def _silu(x):
    return torch.nn.functional.silu(x)
def _sigmoid(x):
    return torch.sigmoid(x)
def _layernorm(x, axis=-1, eps=1e-5):
    m = x.mean(dim=axis, keepdim=True)
    v = x.var(dim=axis, keepdim=True, unbiased=False)
    return (x - m) / torch.sqrt(v + eps)
def _scatter_add(tgt, idx, src, axis=0):
    return tgt.clone().index_add_(axis, idx, src)
def _segment_sum(src, seg, n):
    out = torch.zeros((n,) + src.shape[1:], dtype=src.dtype)
    return out.index_add_(0, seg, src)
def _pad(x, pw):
    flat = tuple(v for pair in reversed(pw) for v in pair)
    return torch.nn.functional.pad(x, flat)

kernel_name = 'standalone__mha_softmax__near_ridge'

def setup_inputs():

    _w = (lambda a: (lambda t: t.to(torch.bfloat16) if t.is_floating_point() else t)(torch.as_tensor(a)))
    B, H, Sq, Sk, D = 8, 32, 256, 512, 128
    return {
        "q": _w((lambda *s: torch.randn(*s, dtype=torch.bfloat16) * 0.8)(B, H, Sq, D)),
        "k": _w((lambda *s: torch.randn(*s, dtype=torch.bfloat16) * 0.8)(B, H, Sk, D)),
        "v": _w((lambda *s: torch.randn(*s, dtype=torch.bfloat16) * 0.05)(B, H, Sk, D)),
    }

def reference(q, k, v):
    scale = q.shape[-1] ** -0.5
    scores = torch.einsum("bhqd,bhkd->bhqk", q, k) * scale
    p = _softmax(scores, axis=-1)
    return torch.einsum("bhqk,bhkd->bhqd", p, v)
```

```cpp
#include <hip/hip_runtime.h>
#include <hip/hip_bf16.h>
using bf16 = __hip_bfloat16;
constexpr int   D = 128, NW = 8, QBLK = 32, KVBLK = 64;
constexpr float SCALE = 0.088388347648318440f;
constexpr float THR = 8.f;
constexpr int SDEPTH = 2;
constexpr int B = 8, HQ = 32, S = 256, SKV = 512;
constexpr int LDQ = D, LDK = D, LDO = D;
constexpr size_t SHM_V = KVBLK * D * 2, SHM_K = KVBLK * D * 2, SHM_ATTN = 2 * SHM_V + 2 * SHM_K + NW * 64 * 4;
using bf16x8 = __attribute__((ext_vector_type(8))) short;
using s16x4  = __attribute__((ext_vector_type(4))) short;
using f32x16 = __attribute__((ext_vector_type(16))) float;
using f32x8  = __attribute__((ext_vector_type(8))) float;
using u32x4  = __attribute__((ext_vector_type(4))) unsigned;
#define KSWZ(row, colB) ((row) * 256 + ((colB) ^ (((row) & 7) << 4)))
#define SBAR() __builtin_amdgcn_sched_barrier(0)
__device__ __forceinline__ int crow(int r, int hi) { return (r & 3) + 8 * (r >> 2) + 4 * hi; }
__device__ __forceinline__ unsigned cvtpk(float lo, float hi) {
  unsigned r; asm volatile("v_cvt_pk_bf16_f32 %0, %1, %2" : "=v"(r) : "v"(lo), "v"(hi)); return r;
}
__device__ static __forceinline__ bf16x8 ld8(const bf16* p) { return *reinterpret_cast<const bf16x8*>(p); }
__device__ __forceinline__ void partialSM(f32x16& p0, f32x16& p1, float& m_reg, float& mn, float& alpha) {
  constexpr float C = SCALE * 1.4426950408889634f;
  float pmax = p0[0]; for (int r = 1; r < 16; ++r) pmax = fmaxf(pmax, p0[r]); for (int r = 0; r < 16; ++r) pmax = fmaxf(pmax, p1[r]);
  { auto rr = __builtin_amdgcn_permlane32_swap(__float_as_uint(pmax), __float_as_uint(pmax), false, false);
    pmax = fmaxf(__uint_as_float(rr[0]), __uint_as_float(rr[1])); }
  if (__builtin_expect(__all(pmax - m_reg <= THR / SCALE), 1)) { mn = m_reg; alpha = 1.f; }
  else { mn = fmaxf(m_reg, pmax); alpha = __builtin_amdgcn_exp2f((m_reg - mn) * C); m_reg = mn; }
  float mnC = -mn * C;
  for (int r = 0; r < 16; ++r) p0[r] = fmaf(p0[r], C, mnC); for (int r = 0; r < 16; ++r) p1[r] = fmaf(p1[r], C, mnC);
  for (int r = 0; r < 16; ++r) p0[r] = __builtin_amdgcn_exp2f(p0[r]);
}
__device__ __forceinline__ void finishSM(f32x16& p0, f32x16& p1, float alpha, float& l_reg, bf16x8& pa0, bf16x8& pa1, bf16x8& pa2, bf16x8& pa3) {
  for (int r = 0; r < 16; ++r) p1[r] = __builtin_amdgcn_exp2f(p1[r]);
  float ps = 0; for (int r = 0; r < 16; ++r) ps += p0[r]; for (int r = 0; r < 16; ++r) ps += p1[r];
  { auto rr = __builtin_amdgcn_permlane32_swap(__float_as_uint(ps), __float_as_uint(ps), false, false);
    ps = __uint_as_float(rr[0]) + __uint_as_float(rr[1]); }
  l_reg = l_reg * alpha + ps;
#define PK4(P, BASE, OUT) do { unsigned a0 = cvtpk(P[BASE + 0], P[BASE + 1]), a1 = cvtpk(P[BASE + 2], P[BASE + 3]);   \
    unsigned b0 = cvtpk(P[BASE + 4], P[BASE + 5]), b1 = cvtpk(P[BASE + 6], P[BASE + 7]);                              \
    auto r0 = __builtin_amdgcn_permlane32_swap(a0, b0, false, false); auto r1 = __builtin_amdgcn_permlane32_swap(a1, b1, false, false); \
    u32x4 w = {r0[0], r1[0], r0[1], r1[1]}; OUT = *reinterpret_cast<bf16x8*>(&w); } while (0)
  PK4(p0, 0, pa0); PK4(p0, 8, pa1); PK4(p1, 0, pa2); PK4(p1, 8, pa3);
#undef PK4
}
__device__ __forceinline__ void qkt(f32x16& p0, f32x16& p1, const bf16* Ks, const bf16x8* qr, int r32, int hi) {
  p0 = f32x16{}; p1 = f32x16{};
  for (int d0 = 0; d0 < 8; ++d0) { int cb = (d0 * 16 + hi * 8) * 2;
    bf16x8 b0 = *reinterpret_cast<const bf16x8*>((const char*)Ks + KSWZ(r32, cb));
    bf16x8 b1 = *reinterpret_cast<const bf16x8*>((const char*)Ks + KSWZ(32 + r32, cb));
    p0 = __builtin_amdgcn_mfma_f32_32x32x16_bf16(b0, qr[d0], p0, 0, 0, 0);
    p1 = __builtin_amdgcn_mfma_f32_32x32x16_bf16(b1, qr[d0], p1, 0, 0, 0); }
}
__device__ __forceinline__ int v_st(int k, int c) { const int kk = (k & ~0xC) | ((k & 4) << 1) | ((k & 8) >> 1); return ((kk >> 3) * 4 + (c >> 5)) * 512 + ((kk & 7) * 32 + (c & 31)) * 2; }
__device__ __forceinline__ int v_rd_base(int lane) { return ((lane & 3) << 3) | (((lane >> 2) & 3) << 6) | (((lane >> 4) & 1) << 5) | (((lane >> 5) & 1) << 8); }
constexpr int v_rd_off(int d0, int ks, int half) { return d0 * 512 + ks * 4096 + half * 2048; }
template <int OFF> __device__ __forceinline__ s16x4 tr_read(int vb) {
  s16x4 r; asm volatile("ds_read_b64_tr_b16 %0, %1 offset:%2" : "=&v"(r) : "v"(vb), "i"(OFF) : "memory"); return r;
}
template <int D0> __device__ __forceinline__ void pv_one(f32x16& od, int vb, bf16x8 pa0, bf16x8 pa1, bf16x8 pa2, bf16x8 pa3) {
  const s16x4 l0 = tr_read<v_rd_off(D0, 0, 0)>(vb), h0 = tr_read<v_rd_off(D0, 0, 1)>(vb), l1 = tr_read<v_rd_off(D0, 1, 0)>(vb), h1 = tr_read<v_rd_off(D0, 1, 1)>(vb);
  const s16x4 l2 = tr_read<v_rd_off(D0, 2, 0)>(vb), h2 = tr_read<v_rd_off(D0, 2, 1)>(vb), l3 = tr_read<v_rd_off(D0, 3, 0)>(vb), h3 = tr_read<v_rd_off(D0, 3, 1)>(vb);
  asm volatile("s_waitcnt lgkmcnt(0)" ::: "memory"); SBAR();
#define PK(L, H) (bf16x8){L[0], L[1], L[2], L[3], H[0], H[1], H[2], H[3]}
  od = __builtin_amdgcn_mfma_f32_32x32x16_bf16(pa0, PK(l0, h0), od, 0, 0, 0);
  od = __builtin_amdgcn_mfma_f32_32x32x16_bf16(pa1, PK(l1, h1), od, 0, 0, 0);
  od = __builtin_amdgcn_mfma_f32_32x32x16_bf16(pa2, PK(l2, h2), od, 0, 0, 0);
  od = __builtin_amdgcn_mfma_f32_32x32x16_bf16(pa3, PK(l3, h3), od, 0, 0, 0);
#undef PK
}
__device__ __forceinline__ void pv_d0(f32x16* o, int vb, bf16x8 pa0, bf16x8 pa1, bf16x8 pa2, bf16x8 pa3) {
  pv_one<0>(o[0], vb, pa0, pa1, pa2, pa3); pv_one<1>(o[1], vb, pa0, pa1, pa2, pa3); pv_one<2>(o[2], vb, pa0, pa1, pa2, pa3); pv_one<3>(o[3], vb, pa0, pa1, pa2, pa3);
}
__device__ __forceinline__ void attn_dense_body(const bf16* __restrict__ Qb, const bf16* __restrict__ Kh, const bf16* __restrict__ Vh,
                                                bf16* __restrict__ Ob, int seq, char* lds) {
  const int tid = threadIdx.x, wid = tid >> 6, lane = tid & 63, r32 = lane & 31, hi = lane >> 5;
  bf16* V_lds = (bf16*)lds; bf16* K_lds = (bf16*)(lds + 2 * SHM_V);
  float* ws = (float*)(lds + 2 * SHM_V + 2 * SHM_K) + wid * 64; float* li_l = ws; float* al_l = ws + 32;
  float m_reg = -1e30f, l_reg = 0; f32x16 o[4] = {}; bf16x8 qr[8];
  const bf16* Qw = Qb + (long)(wid * QBLK + r32) * LDQ + hi * 8;
#pragma unroll
  for (int d0 = 0; d0 < 8; ++d0) qr[d0] = ld8(Qw + d0 * 16);
  const int sr = tid >> 4, sc = (tid & 15) * 8, vst0 = v_st(sr, sc), vst1 = v_st(32 + sr, sc);
  const int vb0 = (int)(uintptr_t)V_lds + v_rd_base(lane);
  struct { bf16x8 vs0, vs1, ks0, ks1; } sr_[SDEPTH];
#define SLOAD(i, k0) do { sr_[i].vs0 = ld8(&Vh[(long)((k0) + sr) * LDK + sc]); sr_[i].vs1 = ld8(&Vh[(long)((k0) + 32 + sr) * LDK + sc]); \
    sr_[i].ks0 = ld8(&Kh[(long)((k0) + sr) * LDK + sc]); sr_[i].ks1 = ld8(&Kh[(long)((k0) + 32 + sr) * LDK + sc]); } while (0)
#define SWRITE(b, i) do { *(bf16x8*)((char*)V_lds + (b) * SHM_V + vst0) = sr_[i].vs0;          \
    *(bf16x8*)((char*)V_lds + (b) * SHM_V + vst1) = sr_[i].vs1; int kc = sc * 2;               \
    *(bf16x8*)((char*)K_lds + (b) * SHM_K + KSWZ(sr, kc)) = sr_[i].ks0;                       \
    *(bf16x8*)((char*)K_lds + (b) * SHM_K + KSWZ(32 + sr, kc)) = sr_[i].ks1; } while (0)
#define SWAIT() do { asm volatile("s_waitcnt vmcnt(4)" ::: "memory"); } while (0)
#define RESC(a) do { if (__any((a) < 1.f)) { if (hi == 0) al_l[r32] = (a); asm volatile("s_waitcnt lgkmcnt(0)" ::: "memory"); \
    for (int d = 0; d < 4; ++d) for (int r = 0; r < 16; ++r) o[d][r] *= al_l[crow(r, hi)]; } } while (0)
  f32x16 pA0, pA1, pB0, pB1; float mnA, mnB, alA, alB; bf16x8 pa0, pa1, pa2, pa3; const int NT = seq / KVBLK;
  constexpr int SE = 0, SO = SDEPTH - 1;
  SLOAD(SE, 0); asm volatile("s_waitcnt vmcnt(0)" ::: "memory"); SWRITE(0, SE); __syncthreads();
  qkt(pA0, pA1, K_lds, qr, r32, hi); partialSM(pA0, pA1, m_reg, mnA, alA);
  SLOAD(SO, KVBLK); if (2 < NT) SLOAD(SE, 2 * KVBLK);
  SWAIT(); SWRITE(1, SO); __syncthreads();
  for (int j = 1; j + 1 < NT; j += 2) {
    SBAR(); qkt(pB0, pB1, (bf16*)((char*)K_lds + SHM_K), qr, r32, hi);
    finishSM(pA0, pA1, alA, l_reg, pa0, pa1, pa2, pa3); SBAR();
    SLOAD(SO, (j + SDEPTH) * KVBLK); SBAR();
    pv_d0(o, vb0, pa0, pa1, pa2, pa3); partialSM(pB0, pB1, m_reg, mnB, alB);
    __syncthreads(); SWAIT(); SWRITE(0, SE);
    RESC(alB); __syncthreads();
    SBAR(); qkt(pA0, pA1, K_lds, qr, r32, hi);
    finishSM(pB0, pB1, alB, l_reg, pa0, pa1, pa2, pa3); SBAR();
    if (j + 3 < NT) SLOAD(SE, (j + 1 + SDEPTH) * KVBLK); SBAR();
    pv_d0(o, vb0 + (int)SHM_V, pa0, pa1, pa2, pa3); partialSM(pA0, pA1, m_reg, mnA, alA);
    __syncthreads(); SWAIT(); SWRITE(1, SO);
    RESC(alA); __syncthreads();
  }
  SBAR(); qkt(pB0, pB1, (bf16*)((char*)K_lds + SHM_K), qr, r32, hi);
  finishSM(pA0, pA1, alA, l_reg, pa0, pa1, pa2, pa3); SBAR();
  pv_d0(o, vb0, pa0, pa1, pa2, pa3); partialSM(pB0, pB1, m_reg, mnB, alB);
  __syncthreads(); RESC(alB);
  finishSM(pB0, pB1, alB, l_reg, pa0, pa1, pa2, pa3); SBAR();
  pv_d0(o, vb0 + (int)SHM_V, pa0, pa1, pa2, pa3);
  if (hi == 0) li_l[r32] = l_reg; asm volatile("s_waitcnt lgkmcnt(0)" ::: "memory");
  float rli[16];
#pragma unroll
  for (int r = 0; r < 16; ++r) rli[r] = __builtin_amdgcn_rcpf(li_l[crow(r, hi)]);
  bf16* Ow = Ob + (long)(wid * QBLK) * LDO;
#pragma unroll
  for (int r = 0; r < 16; ++r) { int orow = crow(r, hi);
    for (int d0 = 0; d0 < 4; ++d0) Ow[(long)orow * LDO + d0 * 32 + r32] = __float2bfloat16(o[d0][r] * rli[r]); }
}
__global__ __launch_bounds__(64 * NW, 1)
void attn_fwd_dense(const bf16* __restrict__ Q, const bf16* __restrict__ K, const bf16* __restrict__ V, bf16* __restrict__ O) {
  extern __shared__ __attribute__((aligned(16))) char shm[];
  const long bh = blockIdx.x;
  attn_dense_body(Q + bh * S * D, K + bh * SKV * D, V + bh * SKV * D, O + bh * S * D, SKV, shm);
}
extern "C" void kernel_launch(void* const* d_in, const int* in_sizes, int n_in, void* d_out, int out_size, void* d_ws, size_t ws_size, hipStream_t stream) {
  hipFuncSetAttribute((const void*)attn_fwd_dense, hipFuncAttributeMaxDynamicSharedMemorySize, (int)SHM_ATTN);
  hipLaunchKernelGGL(attn_fwd_dense, dim3(B * HQ), dim3(64 * NW), SHM_ATTN, stream,
                     (const bf16*)d_in[0], (const bf16*)d_in[1], (const bf16*)d_in[2], (bf16*)d_out);
}
```

```cpp
#include <hip/hip_runtime.h>
#include <hip/hip_bf16.h>


__device__ __forceinline__ float bf_lo(unsigned w) { return __uint_as_float(w << 16); }
__device__ __forceinline__ float bf_hi(unsigned w) { return __uint_as_float(w & 0xffff0000u); }

__device__ __forceinline__ void slow_rows(const unsigned* __restrict__ q, const unsigned* __restrict__ k, const unsigned* __restrict__ v,
                                       unsigned* __restrict__ o) {
  const float scale = 0.08838834764831845f;
  float m = -INFINITY;
  for (int j = 0; j < 512; ++j) {
    float s = 0.f;
    for (int d = 0; d < 64; ++d) { unsigned a = q[d], b = k[j * 64 + d]; s = fmaf(bf_lo(a), bf_lo(b), s); s = fmaf(bf_hi(a), bf_hi(b), s); }
    m = fmaxf(m, s * scale);
  }
  for (int c = 0; c < 8; ++c) {
    float acc0 = 0.f, acc1 = 0.f, acc2 = 0.f, acc3 = 0.f, acc4 = 0.f, acc5 = 0.f, acc6 = 0.f, acc7 = 0.f;
    float acc8 = 0.f, acc9 = 0.f, acc10 = 0.f, acc11 = 0.f, acc12 = 0.f, acc13 = 0.f, acc14 = 0.f, acc15 = 0.f, l = 0.f;
    for (int j = 0; j < 512; ++j) {
      float s = 0.f;
      for (int d = 0; d < 64; ++d) { unsigned a = q[d], b = k[j * 64 + d]; s = fmaf(bf_lo(a), bf_lo(b), s); s = fmaf(bf_hi(a), bf_hi(b), s); }
      const float p = __expf(s * scale - m);
      l += p;
      const unsigned* vr = v + j * 64 + c * 8;
      unsigned w0 = vr[0], w1 = vr[1], w2 = vr[2], w3 = vr[3], w4 = vr[4], w5 = vr[5], w6 = vr[6], w7 = vr[7];
      acc0 = fmaf(p, bf_lo(w0), acc0); acc1 = fmaf(p, bf_hi(w0), acc1); acc2 = fmaf(p, bf_lo(w1), acc2); acc3 = fmaf(p, bf_hi(w1), acc3);
      acc4 = fmaf(p, bf_lo(w2), acc4); acc5 = fmaf(p, bf_hi(w2), acc5); acc6 = fmaf(p, bf_lo(w3), acc6); acc7 = fmaf(p, bf_hi(w3), acc7);
      acc8 = fmaf(p, bf_lo(w4), acc8); acc9 = fmaf(p, bf_hi(w4), acc9); acc10 = fmaf(p, bf_lo(w5), acc10); acc11 = fmaf(p, bf_hi(w5), acc11);
      acc12 = fmaf(p, bf_lo(w6), acc12); acc13 = fmaf(p, bf_hi(w6), acc13); acc14 = fmaf(p, bf_lo(w7), acc14); acc15 = fmaf(p, bf_hi(w7), acc15);
    }
    const float r = 1.f / l;
    auto pk = [&](float a, float b) { __hip_bfloat16 x = __float2bfloat16(a * r), y = __float2bfloat16(b * r);
      return (unsigned)__bfloat16_as_ushort(x) | ((unsigned)__bfloat16_as_ushort(y) << 16); };
    unsigned* op = o + c * 8;
    op[0] = pk(acc0, acc1); op[1] = pk(acc2, acc3); op[2] = pk(acc4, acc5); op[3] = pk(acc6, acc7);
    op[4] = pk(acc8, acc9); op[5] = pk(acc10, acc11); op[6] = pk(acc12, acc13); op[7] = pk(acc14, acc15);
  }
}

__global__ __launch_bounds__(256, 1) void attn_fwd_kernel(const void* __restrict__ Q, const void* __restrict__ K, const void* __restrict__ V,
                                                          void* __restrict__ O
) {
  __shared__ __attribute__((aligned(1024))) char lds[163840];
  unsigned flag;
  asm volatile(
      "v_and_b32 v4, 63, %[tid]\n\t"
      "v_lshrrev_b32 v5, 6, %[tid]\n\t"
      "s_lshl_b32 s42, %[bh], 16\n\t"
      "v_readfirstlane_b32 s36, v5\n\t"
      "s_lshl_b32 s43, %[bh], 17\n\t"
      "s_mov_b64 s[20:21], %[q]\n\t"
      "s_add_u32 s20, s20, s42\n\t"
      "s_addc_u32 s21, s21, 0\n\t"
      "s_and_b32 s21, s21, 0xffff\n\t"
      "s_mov_b32 s22, 65536\n\t"
      "s_mov_b32 s23, 0x20000\n\t"
      "v_lshrrev_b32 v6, 4, v4\n\t"
      "v_and_b32 v7, 15, v4\n\t"
      "v_xor_b32 v9, v7, v6\n\t"
      "v_lshlrev_b32 v10, 8, v6\n\t"
      "v_lshl_add_u32 v12, v9, 4, v10\n\t"
      "v_xor_b32 v11, 4, v9\n\t"
      "v_lshl_add_u32 v13, v11, 4, v10\n\t"
      "v_xor_b32 v11, 8, v9\n\t"
      "v_lshl_add_u32 v14, v11, 4, v10\n\t"
      "v_xor_b32 v11, 12, v9\n\t"
      "v_lshl_add_u32 v15, v11, 4, v10\n\t"
      "s_lshl_b32 s55, s36, 14\n\t"
      "s_add_u32 s54, s55, %[lds]\n\t"
      "s_add_u32 s54, s54, 98304\n\t"
      "s_add_u32 m0, s54, 0\n\t"
      "s_add_u32 s44, s55, 0\n\t"
      "buffer_load_dwordx4 v12, s[20:23], s44 offen offset:0 nt lds\n\t"
      "buffer_load_dwordx4 v13, s[20:23], s44 offen offset:1024 nt lds\n\t"
      "buffer_load_dwordx4 v14, s[20:23], s44 offen offset:2048 nt lds\n\t"
      "buffer_load_dwordx4 v15, s[20:23], s44 offen offset:3072 nt lds\n\t"
      "s_add_u32 m0, s54, 4096\n\t"
      "s_add_u32 s44, s55, 4096\n\t"
      "buffer_load_dwordx4 v12, s[20:23], s44 offen offset:0 nt lds\n\t"
      "buffer_load_dwordx4 v13, s[20:23], s44 offen offset:1024 nt lds\n\t"
      "buffer_load_dwordx4 v14, s[20:23], s44 offen offset:2048 nt lds\n\t"
      "buffer_load_dwordx4 v15, s[20:23], s44 offen offset:3072 nt lds\n\t"
      "s_mov_b64 s[24:25], %[k]\n\t"
      "s_add_u32 s24, s24, s43\n\t"
      "s_addc_u32 s25, s25, 0\n\t"
      "s_and_b32 s25, s25, 0xffff\n\t"
      "s_mov_b32 s26, 131072\n\t"
      "s_mov_b32 s27, 0x20000\n\t"
      "s_and_b32 s45, s36, 1\n\t"
      "s_lshl_b32 s45, s45, 3\n\t"
      "s_lshl_b32 s44, s36, 11\n\t"
      "v_xor_b32 v11, s45, v9\n\t"
      "v_lshl_add_u32 v32, v11, 4, v10\n\t"
      "v_xor_b32 v11, 4, v11\n\t"
      "v_lshl_add_u32 v33, v11, 4, v10\n\t"
      "v_add_u32 v32, s44, v32\n\t"
      "v_add_u32 v33, s44, v33\n\t"
      "s_add_u32 s37, s44, %[lds]\n\t"
      "s_add_u32 s38, s37, 49152\n\t"
      "s_mov_b32 s71, 0\n\t"
      "s_mov_b32 s39, s71\n\t"
      "s_mov_b32 s40, s71\n\t"
      "s_add_u32 m0, s37, 0\n\t"
      "s_nop 0\n\t"
      "buffer_load_dwordx4 v32, s[24:27], s39 offen nt lds\n\t"
      "buffer_load_dwordx4 v33, s[24:27], s39 offen offset:1024 nt lds\n\t"
      "s_add_u32 s39, s39, 8192\n\t"
      "s_getpc_b64 s[46:47]\n\t"
      "s_lshr_b32 s45, %[bh], 3\n\t"
      "s_and_b32 s45, s45, 31\n\t"
      "s_mul_i32 s45, s45, 576\n\t"
      "s_add_u32 s46, s46, s45\n\t"
      "s_addc_u32 s47, s47, 0\n\t"
      "v_and_b32 v16, 15, v4\n\t"
      "v_lshlrev_b32 v16, 6, v16\n\t"
      "v_min_u32 v16, 512, v16\n\t"
      "global_load_dword a200, v16, s[46:47]\n\t"
      "s_mov_b64 s[28:29], %[v]\n\t"
      "s_add_u32 s28, s28, s43\n\t"
      "s_addc_u32 s29, s29, 0\n\t"
      "s_and_b32 s29, s29, 0xffff\n\t"
      "s_mov_b32 s30, 131072\n\t"
      "s_mov_b32 s31, 0x20000\n\t"
      "s_mov_b64 s[56:57], %[o]\n\t"
      "s_add_u32 s56, s56, s42\n\t"
      "s_addc_u32 s57, s57, 0\n\t"
      "s_and_b32 s57, s57, 0xffff\n\t"
      "s_mov_b32 s58, 65536\n\t"
      "s_mov_b32 s59, 0x20000\n\t"
      "s_mov_b64 s[60:61], s[24:25]\n\t"
      "s_mov_b32 s62, 0\n\t"
      "s_mov_b32 s63, 0x20000\n\t"
      "s_mov_b64 s[64:65], s[28:29]\n\t"
      "s_mov_b32 s66, 0\n\t"
      "s_mov_b32 s67, 0x20000\n\t"
      "s_mov_b32 s68, 196608\n\t"
      "s_mov_b32 s69, 196608\n\t"
      "s_mov_b32 s70, 0\n\t"
      "v_lshlrev_b32 v11, 1, v6\n\t"
      "v_xor_b32 v11, v7, v11\n\t"
      "v_lshl_add_u32 v34, v11, 4, v10\n\t"
      "v_xor_b32 v11, 8, v11\n\t"
      "v_lshl_add_u32 v35, v11, 4, v10\n\t"
      "v_add_u32 v34, s44, v34\n\t"
      "v_add_u32 v35, s44, v35\n\t"
      "v_lshlrev_b32 v19, 8, v7\n\t"
      "v_lshl_add_u32 v19, v9, 4, v19\n\t"
      "v_mov_b32 v44, v19\n\t"
      "v_xor_b32 v45, 64, v19\n\t"
      "v_xor_b32 v46, 128, v19\n\t"
      "v_xor_b32 v47, 192, v19\n\t"
      "v_add_u32 v44, %[lds], v44\n\t"
      "v_add_u32 v45, %[lds], v45\n\t"
      "v_add_u32 v46, %[lds], v46\n\t"
      "v_add_u32 v47, %[lds], v47\n\t"
      "s_add_u32 s42, s55, 98304\n\t"
      "v_add_u32 v20, s42, v44\n\t"
      "v_add_u32 v21, s42, v45\n\t"
      "v_add_u32 v22, s42, v46\n\t"
      "v_add_u32 v23, s42, v47\n\t"
      "v_bfe_u32 v24, v4, 2, 2\n\t"
      "v_bfe_u32 v25, v4, 1, 1\n\t"
      "v_and_b32 v26, 1, v6\n\t"
      "v_lshl_add_u32 v26, v26, 2, v24\n\t"
      "v_lshlrev_b32 v26, 1, v26\n\t"
      "v_xor_b32 v26, v26, v25\n\t"
      "v_lshl_add_u32 v27, v6, 2, v24\n\t"
      "v_lshlrev_b32 v27, 8, v27\n\t"
      "v_lshl_add_u32 v27, v26, 4, v27\n\t"
      "v_and_b32 v26, 1, v4\n\t"
      "v_lshl_add_u32 v27, v26, 3, v27\n\t"
      "s_add_u32 s42, %[lds], 49152\n\t"
      "v_mov_b32 v36, v27\n\t"
      "v_xor_b32 v37, 32, v27\n\t"
      "v_xor_b32 v38, 64, v27\n\t"
      "v_xor_b32 v39, 96, v27\n\t"
      "v_xor_b32 v40, 128, v27\n\t"
      "v_xor_b32 v41, 160, v27\n\t"
      "v_xor_b32 v42, 192, v27\n\t"
      "v_xor_b32 v43, 224, v27\n\t"
      "v_add_u32 v36, s42, v36\n\t"
      "v_add_u32 v37, s42, v37\n\t"
      "v_add_u32 v38, s42, v38\n\t"
      "v_add_u32 v39, s42, v39\n\t"
      "v_add_u32 v40, s42, v40\n\t"
      "v_add_u32 v41, s42, v41\n\t"
      "v_add_u32 v42, s42, v42\n\t"
      "v_add_u32 v43, s42, v43\n\t"
      "s_mov_b32 s48, 0x3e0293ee\n\t"
      "s_mov_b32 s49, 0x3e0293ee\n\t"
      "s_mov_b32 s50, 0xffff0000\n\t"
      "s_mov_b32 s51, 0x5d800000\n\t"
      "s_mov_b32 s41, 0\n\t"
      "s_mov_b32 s52, 0\n\t"
      "s_mov_b32 s53, 0\n\t"
      "v_mov_b32 v128, 0\n\t"
      "v_mov_b32 v129, 0\n\t"
      "v_mov_b32 v130, 0\n\t"
      "v_mov_b32 v131, 0\n\t"
      "v_mov_b32 v132, 0\n\t"
      "v_mov_b32 v133, 0\n\t"
      "v_mov_b32 v134, 0\n\t"
      "v_mov_b32 v135, 0\n\t"
      "v_mov_b32 v136, 0\n\t"
      "v_mov_b32 v137, 0\n\t"
      "v_mov_b32 v138, 0\n\t"
      "v_mov_b32 v139, 0\n\t"
      "v_mov_b32 v140, 0\n\t"
      "v_mov_b32 v141, 0\n\t"
      "v_mov_b32 v142, 0\n\t"
      "v_mov_b32 v143, 0\n\t"
      "v_mov_b32 v144, 0\n\t"
      "v_mov_b32 v145, 0\n\t"
      "v_mov_b32 v146, 0\n\t"
      "v_mov_b32 v147, 0\n\t"
      "v_mov_b32 v148, 0\n\t"
      "v_mov_b32 v149, 0\n\t"
      "v_mov_b32 v150, 0\n\t"
      "v_mov_b32 v151, 0\n\t"
      "v_mov_b32 v152, 0\n\t"
      "v_mov_b32 v153, 0\n\t"
      "v_mov_b32 v154, 0\n\t"
      "v_mov_b32 v155, 0\n\t"
      "v_mov_b32 v156, 0\n\t"
      "v_mov_b32 v157, 0\n\t"
      "v_mov_b32 v158, 0\n\t"
      "v_mov_b32 v159, 0\n\t"
      "v_mov_b32 v160, 0\n\t"
      "v_mov_b32 v161, 0\n\t"
      "v_mov_b32 v162, 0\n\t"
      "v_mov_b32 v163, 0\n\t"
      "v_mov_b32 v164, 0\n\t"
      "v_mov_b32 v165, 0\n\t"
      "v_mov_b32 v166, 0\n\t"
      "v_mov_b32 v167, 0\n\t"
      "v_mov_b32 v168, 0\n\t"
      "v_mov_b32 v169, 0\n\t"
      "v_mov_b32 v170, 0\n\t"
      "v_mov_b32 v171, 0\n\t"
      "v_mov_b32 v172, 0\n\t"
      "v_mov_b32 v173, 0\n\t"
      "v_mov_b32 v174, 0\n\t"
      "v_mov_b32 v175, 0\n\t"
      "v_mov_b32 v176, 0\n\t"
      "v_mov_b32 v177, 0\n\t"
      "v_mov_b32 v178, 0\n\t"
      "v_mov_b32 v179, 0\n\t"
      "v_mov_b32 v180, 0\n\t"
      "v_mov_b32 v181, 0\n\t"
      "v_mov_b32 v182, 0\n\t"
      "v_mov_b32 v183, 0\n\t"
      "v_mov_b32 v184, 0\n\t"
      "v_mov_b32 v185, 0\n\t"
      "v_mov_b32 v186, 0\n\t"
      "v_mov_b32 v187, 0\n\t"
      "v_mov_b32 v188, 0\n\t"
      "v_mov_b32 v189, 0\n\t"
      "v_mov_b32 v190, 0\n\t"
      "v_mov_b32 v191, 0\n\t"
      "v_mov_b32 v192, 0\n\t"
      "v_mov_b32 v193, 0\n\t"
      "v_mov_b32 v194, 0\n\t"
      "v_mov_b32 v195, 0\n\t"
      "v_mov_b32 v196, 0\n\t"
      "v_mov_b32 v197, 0\n\t"
      "v_mov_b32 v198, 0\n\t"
      "v_mov_b32 v199, 0\n\t"
      "v_mov_b32 v200, 0\n\t"
      "v_mov_b32 v201, 0\n\t"
      "v_mov_b32 v202, 0\n\t"
      "v_mov_b32 v203, 0\n\t"
      "v_mov_b32 v204, 0\n\t"
      "v_mov_b32 v205, 0\n\t"
      "v_mov_b32 v206, 0\n\t"
      "v_mov_b32 v207, 0\n\t"
      "v_mov_b32 v208, 0\n\t"
      "v_mov_b32 v209, 0\n\t"
      "v_mov_b32 v210, 0\n\t"
      "v_mov_b32 v211, 0\n\t"
      "v_mov_b32 v212, 0\n\t"
      "v_mov_b32 v213, 0\n\t"
      "v_mov_b32 v214, 0\n\t"
      "v_mov_b32 v215, 0\n\t"
      "v_mov_b32 v216, 0\n\t"
      "v_mov_b32 v217, 0\n\t"
      "v_mov_b32 v218, 0\n\t"
      "v_mov_b32 v219, 0\n\t"
      "v_mov_b32 v220, 0\n\t"
      "v_mov_b32 v221, 0\n\t"
      "v_mov_b32 v222, 0\n\t"
      "v_mov_b32 v223, 0\n\t"
      "v_mov_b32 v224, 0\n\t"
      "v_mov_b32 v225, 0\n\t"
      "v_mov_b32 v226, 0\n\t"
      "v_mov_b32 v227, 0\n\t"
      "v_mov_b32 v228, 0\n\t"
      "v_mov_b32 v229, 0\n\t"
      "v_mov_b32 v230, 0\n\t"
      "v_mov_b32 v231, 0\n\t"
      "v_mov_b32 v232, 0\n\t"
      "v_mov_b32 v233, 0\n\t"
      "v_mov_b32 v234, 0\n\t"
      "v_mov_b32 v235, 0\n\t"
      "v_mov_b32 v236, 0\n\t"
      "v_mov_b32 v237, 0\n\t"
      "v_mov_b32 v238, 0\n\t"
      "v_mov_b32 v239, 0\n\t"
      "v_mov_b32 v240, 0\n\t"
      "v_mov_b32 v241, 0\n\t"
      "v_mov_b32 v242, 0\n\t"
      "v_mov_b32 v243, 0\n\t"
      "v_mov_b32 v244, 0\n\t"
      "v_mov_b32 v245, 0\n\t"
      "v_mov_b32 v246, 0\n\t"
      "v_mov_b32 v247, 0\n\t"
      "v_mov_b32 v248, 0\n\t"
      "v_mov_b32 v249, 0\n\t"
      "v_mov_b32 v250, 0\n\t"
      "v_mov_b32 v251, 0\n\t"
      "v_mov_b32 v252, 0\n\t"
      "v_mov_b32 v253, 0\n\t"
      "v_mov_b32 v254, 0\n\t"
      "v_mov_b32 v255, 0\n\t"
      "v_mov_b32 v28, 0\n\t"
      "v_mov_b32 v29, 0\n\t"
      "v_mov_b32 v30, 0\n\t"
      "v_mov_b32 v31, 0\n\t"
      "s_add_u32 m0, s54, 8192\n\t"
      "s_add_u32 s44, s55, 8192\n\t"
      "buffer_load_dwordx4 v12, s[20:23], s44 offen offset:0 nt lds\n\t"
      "buffer_load_dwordx4 v13, s[20:23], s44 offen offset:1024 nt lds\n\t"
      "buffer_load_dwordx4 v14, s[20:23], s44 offen offset:2048 nt lds\n\t"
      "buffer_load_dwordx4 v15, s[20:23], s44 offen offset:3072 nt lds\n\t"
      "s_add_u32 m0, s54, 12288\n\t"
      "s_add_u32 s44, s55, 12288\n\t"
      "buffer_load_dwordx4 v12, s[20:23], s44 offen offset:0 nt lds\n\t"
      "buffer_load_dwordx4 v13, s[20:23], s44 offen offset:1024 nt lds\n\t"
      "buffer_load_dwordx4 v14, s[20:23], s44 offen offset:2048 nt lds\n\t"
      "buffer_load_dwordx4 v15, s[20:23], s44 offen offset:3072 nt lds\n\t"
      "s_waitcnt vmcnt(11)\n\t"
      "ds_read_b128 v[96:99], v20 offset:0\n\t"
      "ds_read_b128 v[100:103], v21 offset:0\n\t"
      "ds_read_b128 v[104:107], v22 offset:0\n\t"
      "ds_read_b128 v[108:111], v23 offset:0\n\t"
      "ds_read_b128 v[112:115], v20 offset:4096\n\t"
      "ds_read_b128 v[116:119], v21 offset:4096\n\t"
      "ds_read_b128 v[120:123], v22 offset:4096\n\t"
      "ds_read_b128 v[124:127], v23 offset:4096\n\t"
      "s_waitcnt lgkmcnt(6)\n\t"
      "v_lshlrev_b32 v4, 16, v96\n\t"
      "v_and_b32 v5, s50, v96\n\t"
      "v_pk_mul_f32 v[4:5], v[4:5], s[48:49]\n\t"
      "v_cvt_pk_bf16_f32 v96, v4, v5\n\t"
      "v_accvgpr_write_b32 a0, v96\n\t"
      "v_lshlrev_b32 v6, 16, v97\n\t"
      "v_and_b32 v7, s50, v97\n\t"
      "v_pk_mul_f32 v[6:7], v[6:7], s[48:49]\n\t"
      "v_cvt_pk_bf16_f32 v97, v6, v7\n\t"
      "v_accvgpr_write_b32 a1, v97\n\t"
      "v_lshlrev_b32 v24, 16, v98\n\t"
      "v_and_b32 v25, s50, v98\n\t"
      "v_pk_mul_f32 v[24:25], v[24:25], s[48:49]\n\t"
      "v_cvt_pk_bf16_f32 v98, v24, v25\n\t"
      "v_accvgpr_write_b32 a2, v98\n\t"
      "v_lshlrev_b32 v26, 16, v99\n\t"
      "v_and_b32 v27, s50, v99\n\t"
      "v_pk_mul_f32 v[26:27], v[26:27], s[48:49]\n\t"
      "v_cvt_pk_bf16_f32 v99, v26, v27\n\t"
      "v_accvgpr_write_b32 a3, v99\n\t"
      "v_lshlrev_b32 v4, 16, v100\n\t"
      "v_and_b32 v5, s50, v100\n\t"
      "v_pk_mul_f32 v[4:5], v[4:5], s[48:49]\n\t"
      "v_cvt_pk_bf16_f32 v100, v4, v5\n\t"
      "v_accvgpr_write_b32 a4, v100\n\t"
      "v_lshlrev_b32 v6, 16, v101\n\t"
      "v_and_b32 v7, s50, v101\n\t"
      "v_pk_mul_f32 v[6:7], v[6:7], s[48:49]\n\t"
      "v_cvt_pk_bf16_f32 v101, v6, v7\n\t"
      "v_accvgpr_write_b32 a5, v101\n\t"
      "v_lshlrev_b32 v24, 16, v102\n\t"
      "v_and_b32 v25, s50, v102\n\t"
      "v_pk_mul_f32 v[24:25], v[24:25], s[48:49]\n\t"
      "v_cvt_pk_bf16_f32 v102, v24, v25\n\t"
      "v_accvgpr_write_b32 a6, v102\n\t"
      "v_lshlrev_b32 v26, 16, v103\n\t"
      "v_and_b32 v27, s50, v103\n\t"
      "v_pk_mul_f32 v[26:27], v[26:27], s[48:49]\n\t"
      "v_cvt_pk_bf16_f32 v103, v26, v27\n\t"
      "v_accvgpr_write_b32 a7, v103\n\t"
      "s_waitcnt lgkmcnt(4)\n\t"
      "v_lshlrev_b32 v4, 16, v104\n\t"
      "v_and_b32 v5, s50, v104\n\t"
      "v_pk_mul_f32 v[4:5], v[4:5], s[48:49]\n\t"
      "v_cvt_pk_bf16_f32 v104, v4, v5\n\t"
      "v_accvgpr_write_b32 a8, v104\n\t"
      "v_lshlrev_b32 v6, 16, v105\n\t"
      "v_and_b32 v7, s50, v105\n\t"
      "v_pk_mul_f32 v[6:7], v[6:7], s[48:49]\n\t"
      "v_cvt_pk_bf16_f32 v105, v6, v7\n\t"
      "v_accvgpr_write_b32 a9, v105\n\t"
      "v_lshlrev_b32 v24, 16, v106\n\t"
      "v_and_b32 v25, s50, v106\n\t"
      "v_pk_mul_f32 v[24:25], v[24:25], s[48:49]\n\t"
      "v_cvt_pk_bf16_f32 v106, v24, v25\n\t"
      "v_accvgpr_write_b32 a10, v106\n\t"
      "v_lshlrev_b32 v26, 16, v107\n\t"
      "v_and_b32 v27, s50, v107\n\t"
      "v_pk_mul_f32 v[26:27], v[26:27], s[48:49]\n\t"
      "v_cvt_pk_bf16_f32 v107, v26, v27\n\t"
      "v_accvgpr_write_b32 a11, v107\n\t"
      "v_lshlrev_b32 v4, 16, v108\n\t"
      "v_and_b32 v5, s50, v108\n\t"
      "v_pk_mul_f32 v[4:5], v[4:5], s[48:49]\n\t"
      "v_cvt_pk_bf16_f32 v108, v4, v5\n\t"
      "v_accvgpr_write_b32 a12, v108\n\t"
      "v_lshlrev_b32 v6, 16, v109\n\t"
      "v_and_b32 v7, s50, v109\n\t"
      "v_pk_mul_f32 v[6:7], v[6:7], s[48:49]\n\t"
      "v_cvt_pk_bf16_f32 v109, v6, v7\n\t"
      "v_accvgpr_write_b32 a13, v109\n\t"
      "v_lshlrev_b32 v24, 16, v110\n\t"
      "v_and_b32 v25, s50, v110\n\t"
      "v_pk_mul_f32 v[24:25], v[24:25], s[48:49]\n\t"
      "v_cvt_pk_bf16_f32 v110, v24, v25\n\t"
      "v_accvgpr_write_b32 a14, v110\n\t"
      "v_lshlrev_b32 v26, 16, v111\n\t"
      "v_and_b32 v27, s50, v111\n\t"
      "v_pk_mul_f32 v[26:27], v[26:27], s[48:49]\n\t"
      "v_cvt_pk_bf16_f32 v111, v26, v27\n\t"
      "v_accvgpr_write_b32 a15, v111\n\t"
      "s_add_u32 m0, s38, 0\n\t"
      "s_nop 0\n\t"
      "buffer_load_dwordx4 v34, s[28:31], s40 offen nt lds\n\t"
      "buffer_load_dwordx4 v35, s[28:31], s40 offen offset:1024 nt lds\n\t"
      "s_add_u32 s40, s40, 8192\n\t"
      "s_waitcnt lgkmcnt(2)\n\t"
      "v_lshlrev_b32 v4, 16, v112\n\t"
      "v_and_b32 v5, s50, v112\n\t"
      "v_pk_mul_f32 v[4:5], v[4:5], s[48:49]\n\t"
      "v_cvt_pk_bf16_f32 v112, v4, v5\n\t"
      "v_accvgpr_write_b32 a16, v112\n\t"
      "v_lshlrev_b32 v6, 16, v113\n\t"
      "v_and_b32 v7, s50, v113\n\t"
      "v_pk_mul_f32 v[6:7], v[6:7], s[48:49]\n\t"
      "v_cvt_pk_bf16_f32 v113, v6, v7\n\t"
      "v_accvgpr_write_b32 a17, v113\n\t"
      "v_lshlrev_b32 v24, 16, v114\n\t"
      "v_and_b32 v25, s50, v114\n\t"
      "v_pk_mul_f32 v[24:25], v[24:25], s[48:49]\n\t"
      "v_cvt_pk_bf16_f32 v114, v24, v25\n\t"
      "v_accvgpr_write_b32 a18, v114\n\t"
      "v_lshlrev_b32 v26, 16, v115\n\t"
      "v_and_b32 v27, s50, v115\n\t"
      "v_pk_mul_f32 v[26:27], v[26:27], s[48:49]\n\t"
      "v_cvt_pk_bf16_f32 v115, v26, v27\n\t"
      "v_accvgpr_write_b32 a19, v115\n\t"
      "v_lshlrev_b32 v4, 16, v116\n\t"
      "v_and_b32 v5, s50, v116\n\t"
      "v_pk_mul_f32 v[4:5], v[4:5], s[48:49]\n\t"
      "v_cvt_pk_bf16_f32 v116, v4, v5\n\t"
      "v_accvgpr_write_b32 a20, v116\n\t"
      "v_lshlrev_b32 v6, 16, v117\n\t"
      "v_and_b32 v7, s50, v117\n\t"
      "v_pk_mul_f32 v[6:7], v[6:7], s[48:49]\n\t"
      "v_cvt_pk_bf16_f32 v117, v6, v7\n\t"
      "v_accvgpr_write_b32 a21, v117\n\t"
      "v_lshlrev_b32 v24, 16, v118\n\t"
      "v_and_b32 v25, s50, v118\n\t"
      "v_pk_mul_f32 v[24:25], v[24:25], s[48:49]\n\t"
      "v_cvt_pk_bf16_f32 v118, v24, v25\n\t"
      "v_accvgpr_write_b32 a22, v118\n\t"
      "v_lshlrev_b32 v26, 16, v119\n\t"
      "v_and_b32 v27, s50, v119\n\t"
      "v_pk_mul_f32 v[26:27], v[26:27], s[48:49]\n\t"
      "v_cvt_pk_bf16_f32 v119, v26, v27\n\t"
      "v_accvgpr_write_b32 a23, v119\n\t"
      "s_waitcnt lgkmcnt(0)\n\t"
      "v_lshlrev_b32 v4, 16, v120\n\t"
      "v_and_b32 v5, s50, v120\n\t"
      "v_pk_mul_f32 v[4:5], v[4:5], s[48:49]\n\t"
      "v_cvt_pk_bf16_f32 v120, v4, v5\n\t"
      "v_accvgpr_write_b32 a24, v120\n\t"
      "v_lshlrev_b32 v6, 16, v121\n\t"
      "v_and_b32 v7, s50, v121\n\t"
      "v_pk_mul_f32 v[6:7], v[6:7], s[48:49]\n\t"
      "v_cvt_pk_bf16_f32 v121, v6, v7\n\t"
      "v_accvgpr_write_b32 a25, v121\n\t"
      "v_lshlrev_b32 v24, 16, v122\n\t"
      "v_and_b32 v25, s50, v122\n\t"
      "v_pk_mul_f32 v[24:25], v[24:25], s[48:49]\n\t"
      "v_cvt_pk_bf16_f32 v122, v24, v25\n\t"
      "v_accvgpr_write_b32 a26, v122\n\t"
      "v_lshlrev_b32 v26, 16, v123\n\t"
      "v_and_b32 v27, s50, v123\n\t"
      "v_pk_mul_f32 v[26:27], v[26:27], s[48:49]\n\t"
      "v_cvt_pk_bf16_f32 v123, v26, v27\n\t"
      "v_accvgpr_write_b32 a27, v123\n\t"
      "v_lshlrev_b32 v4, 16, v124\n\t"
      "v_and_b32 v5, s50, v124\n\t"
      "v_pk_mul_f32 v[4:5], v[4:5], s[48:49]\n\t"
      "v_cvt_pk_bf16_f32 v124, v4, v5\n\t"
      "v_accvgpr_write_b32 a28, v124\n\t"
      "v_lshlrev_b32 v6, 16, v125\n\t"
      "v_and_b32 v7, s50, v125\n\t"
      "v_pk_mul_f32 v[6:7], v[6:7], s[48:49]\n\t"
      "v_cvt_pk_bf16_f32 v125, v6, v7\n\t"
      "v_accvgpr_write_b32 a29, v125\n\t"
      "v_lshlrev_b32 v24, 16, v126\n\t"
      "v_and_b32 v25, s50, v126\n\t"
      "v_pk_mul_f32 v[24:25], v[24:25], s[48:49]\n\t"
      "v_cvt_pk_bf16_f32 v126, v24, v25\n\t"
      "v_accvgpr_write_b32 a30, v126\n\t"
      "v_lshlrev_b32 v26, 16, v127\n\t"
      "v_and_b32 v27, s50, v127\n\t"
      "v_pk_mul_f32 v[26:27], v[26:27], s[48:49]\n\t"
      "v_cvt_pk_bf16_f32 v127, v26, v27\n\t"
      "v_accvgpr_write_b32 a31, v127\n\t"
      "s_waitcnt vmcnt(11)\n\t"
      "s_barrier\n\t"
      "ds_read_b128 a[32:35], v44 offset:0\n\t"
      "ds_read_b128 a[36:39], v45 offset:0\n\t"
      "ds_read_b128 a[40:43], v46 offset:0\n\t"
      "ds_read_b128 a[44:47], v47 offset:0\n\t"
      "ds_read_b128 a[48:51], v44 offset:4096\n\t"
      "ds_read_b128 a[52:55], v45 offset:4096\n\t"
      "ds_read_b128 a[56:59], v46 offset:4096\n\t"
      "ds_read_b128 a[60:63], v47 offset:4096\n\t"
      "s_waitcnt lgkmcnt(0)\n\t"
      "v_mfma_f32_16x16x32_bf16 v[96:99], a[32:35], a[0:3], 0\n\t"
      "v_mfma_f32_16x16x32_bf16 v[96:99], a[36:39], a[4:7], v[96:99]\n\t"
      "v_mfma_f32_16x16x32_bf16 v[96:99], a[40:43], a[8:11], v[96:99]\n\t"
      "v_mfma_f32_16x16x32_bf16 v[96:99], a[44:47], a[12:15], v[96:99]\n\t"
      "v_mfma_f32_16x16x32_bf16 v[100:103], a[48:51], a[0:3], 0\n\t"
      "v_mfma_f32_16x16x32_bf16 v[100:103], a[52:55], a[4:7], v[100:103]\n\t"
      "v_mfma_f32_16x16x32_bf16 v[100:103], a[56:59], a[8:11], v[100:103]\n\t"
      "v_mfma_f32_16x16x32_bf16 v[100:103], a[60:63], a[12:15], v[100:103]\n\t"
      "v_mfma_f32_16x16x32_bf16 v[104:107], a[32:35], a[16:19], 0\n\t"
      "v_mfma_f32_16x16x32_bf16 v[104:107], a[36:39], a[20:23], v[104:107]\n\t"
      "v_mfma_f32_16x16x32_bf16 v[104:107], a[40:43], a[24:27], v[104:107]\n\t"
      "v_mfma_f32_16x16x32_bf16 v[104:107], a[44:47], a[28:31], v[104:107]\n\t"
      "v_mfma_f32_16x16x32_bf16 v[108:111], a[48:51], a[16:19], 0\n\t"
      "v_mfma_f32_16x16x32_bf16 v[108:111], a[52:55], a[20:23], v[108:111]\n\t"
      "v_mfma_f32_16x16x32_bf16 v[108:111], a[56:59], a[24:27], v[108:111]\n\t"
      "v_mfma_f32_16x16x32_bf16 v[108:111], a[60:63], a[28:31], v[108:111]\n\t"
      "s_add_u32 m0, s37, 8192\n\t"
      "s_nop 0\n\t"
      "buffer_load_dwordx4 v32, s[24:27], s39 offen nt lds\n\t"
      "buffer_load_dwordx4 v33, s[24:27], s39 offen offset:1024 nt lds\n\t"
      "s_add_u32 s39, s39, 8192\n\t"
      "s_nop 3\n\t"
      "v_max3_f32 v4, v96, v97, v98\n\t"
      "v_max3_f32 v6, v104, v105, v106\n\t"
      "v_max3_f32 v4, v4, v99, v100\n\t"
      "v_max3_f32 v6, v6, v107, v108\n\t"
      "v_max3_f32 v4, v4, v101, v102\n\t"
      "v_max3_f32 v6, v6, v109, v110\n\t"
      "v_max_f32 v4, v4, v103\n\t"
      "v_max_f32 v6, v6, v111\n\t"
      "v_mov_b32 v5, v4\n\t"
      "v_mov_b32 v7, v6\n\t"
      "s_nop 1\n\t"
      "v_permlane16_swap_b32 v4, v5\n\t"
      "v_permlane16_swap_b32 v6, v7\n\t"
      "s_nop 1\n\t"
      "v_max_f32 v4, v4, v5\n\t"
      "v_max_f32 v6, v6, v7\n\t"
      "v_mov_b32 v5, v4\n\t"
      "v_mov_b32 v7, v6\n\t"
      "s_nop 1\n\t"
      "v_permlane32_swap_b32 v4, v5\n\t"
      "v_permlane32_swap_b32 v6, v7\n\t"
      "s_nop 1\n\t"
      "v_max_f32 v4, v4, v5\n\t"
      "v_max_f32 v6, v6, v7\n\t"
      "v_mul_f32 v80, -1.0, v4\n\t"
      "v_mul_f32 v84, -1.0, v6\n\t"
      "v_mul_f32 v81, -1.0, v4\n\t"
      "v_mul_f32 v85, -1.0, v6\n\t"
      "v_mul_f32 v82, -1.0, v4\n\t"
      "v_mul_f32 v86, -1.0, v6\n\t"
      "v_mul_f32 v83, -1.0, v4\n\t"
      "v_mul_f32 v87, -1.0, v6\n\t"
      "v_sub_f32 v96, v96, v4\n\t"
      "v_sub_f32 v104, v104, v6\n\t"
      "v_sub_f32 v97, v97, v4\n\t"
      "v_sub_f32 v105, v105, v6\n\t"
      "v_sub_f32 v98, v98, v4\n\t"
      "v_sub_f32 v106, v106, v6\n\t"
      "v_sub_f32 v99, v99, v4\n\t"
      "v_sub_f32 v107, v107, v6\n\t"
      "v_sub_f32 v100, v100, v4\n\t"
      "v_sub_f32 v108, v108, v6\n\t"
      "v_sub_f32 v101, v101, v4\n\t"
      "v_sub_f32 v109, v109, v6\n\t"
      "v_sub_f32 v102, v102, v4\n\t"
      "v_sub_f32 v110, v110, v6\n\t"
      "v_sub_f32 v103, v103, v4\n\t"
      "v_sub_f32 v111, v111, v6\n\t"
      "s_add_u32 m0, s37, 16384\n\t"
      "s_nop 0\n\t"
      "buffer_load_dwordx4 v32, s[24:27], s39 offen nt lds\n\t"
      "buffer_load_dwordx4 v33, s[24:27], s39 offen offset:1024 nt lds\n\t"
      "s_add_u32 s39, s39, 8192\n\t"
      "v_exp_f32 v96, v96\n\t"
      "v_exp_f32 v97, v97\n\t"
      "v_exp_f32 v98, v98\n\t"
      "v_exp_f32 v99, v99\n\t"
      "v_exp_f32 v100, v100\n\t"
      "v_exp_f32 v101, v101\n\t"
      "v_exp_f32 v102, v102\n\t"
      "v_exp_f32 v103, v103\n\t"
      "v_exp_f32 v104, v104\n\t"
      "v_exp_f32 v105, v105\n\t"
      "v_exp_f32 v106, v106\n\t"
      "v_exp_f32 v107, v107\n\t"
      "v_exp_f32 v108, v108\n\t"
      "v_exp_f32 v109, v109\n\t"
      "v_exp_f32 v110, v110\n\t"
      "v_exp_f32 v111, v111\n\t"
      "s_nop 0\n\t"
      "v_add_f32 v28, v28, v96\n\t"
      "v_add_f32 v28, v28, v97\n\t"
      "v_add_f32 v28, v28, v98\n\t"
      "v_add_f32 v28, v28, v99\n\t"
      "v_add_f32 v28, v28, v100\n\t"
      "v_add_f32 v28, v28, v101\n\t"
      "v_add_f32 v28, v28, v102\n\t"
      "v_add_f32 v28, v28, v103\n\t"
      "v_add_f32 v29, v29, v104\n\t"
      "v_add_f32 v29, v29, v105\n\t"
      "v_add_f32 v29, v29, v106\n\t"
      "v_add_f32 v29, v29, v107\n\t"
      "v_add_f32 v29, v29, v108\n\t"
      "v_add_f32 v29, v29, v109\n\t"
      "v_add_f32 v29, v29, v110\n\t"
      "v_add_f32 v29, v29, v111\n\t"
      "v_cvt_pk_bf16_f32 v96, v96, v97\n\t"
      "v_cvt_pk_bf16_f32 v97, v98, v99\n\t"
      "v_cvt_pk_bf16_f32 v98, v100, v101\n\t"
      "v_cvt_pk_bf16_f32 v99, v102, v103\n\t"
      "v_cvt_pk_bf16_f32 v104, v104, v105\n\t"
      "v_cvt_pk_bf16_f32 v105, v106, v107\n\t"
      "v_cvt_pk_bf16_f32 v106, v108, v109\n\t"
      "v_cvt_pk_bf16_f32 v107, v110, v111\n\t"
      "v_and_b32 v4, 63, %[tid]\n\t"
      "v_lshrrev_b32 v6, 4, v4\n\t"
      "v_and_b32 v7, 15, v4\n\t"
      "v_lshrrev_b32 v16, 1, v6\n\t"
      "v_and_b32 v17, 7, v7\n\t"
      "v_xor_b32 v16, v16, v17\n\t"
      "v_lshlrev_b32 v19, 8, v7\n\t"
      "v_lshl_add_u32 v19, v16, 4, v19\n\t"
      "v_and_b32 v16, 1, v6\n\t"
      "v_lshl_add_u32 v19, v16, 3, v19\n\t"
      "v_mov_b32 v8, v19\n\t"
      "v_xor_b32 v9, 32, v19\n\t"
      "v_xor_b32 v10, 64, v19\n\t"
      "v_xor_b32 v11, 96, v19\n\t"
      "v_xor_b32 v12, 128, v19\n\t"
      "v_xor_b32 v13, 160, v19\n\t"
      "v_xor_b32 v14, 192, v19\n\t"
      "v_xor_b32 v15, 224, v19\n\t"
      "v_add_u32 v8, s54, v8\n\t"
      "v_add_u32 v9, s54, v9\n\t"
      "v_add_u32 v10, s54, v10\n\t"
      "v_add_u32 v11, s54, v11\n\t"
      "v_add_u32 v12, s54, v12\n\t"
      "v_add_u32 v13, s54, v13\n\t"
      "v_add_u32 v14, s54, v14\n\t"
      "v_add_u32 v15, s54, v15\n\t"
      "v_xor_b32 v19, v7, v6\n\t"
      "v_lshlrev_b32 v18, 8, v6\n\t"
      "v_lshl_add_u32 v16, v19, 4, v18\n\t"
      "v_xor_b32 v19, 4, v19\n\t"
      "v_lshl_add_u32 v17, v19, 4, v18\n\t"
      "v_add_u32 v16, s54, v16\n\t"
      "v_add_u32 v17, s54, v17\n\t"
      "v_lshlrev_b32 v18, 4, v4\n\t"
      "s_waitcnt vmcnt(10)\n\t"
      "ds_read_b128 v[48:51], v20 offset:8192\n\t"
      "ds_read_b128 v[52:55], v21 offset:8192\n\t"
      "ds_read_b128 v[56:59], v22 offset:8192\n\t"
      "ds_read_b128 v[60:63], v23 offset:8192\n\t"
      "s_add_u32 m0, s38, 8192\n\t"
      "s_nop 0\n\t"
      "buffer_load_dwordx4 v34, s[28:31], s40 offen nt lds\n\t"
      "buffer_load_dwordx4 v35, s[28:31], s40 offen offset:1024 nt lds\n\t"
      "s_add_u32 s40, s40, 8192\n\t"
      "s_mov_b32 m0, 196608\n\t"
      "s_nop 0\n\t"
      "buffer_load_dwordx4 v32, s[60:63], s70 offen nt lds\n\t"
      "s_waitcnt lgkmcnt(3)\n\t"
      "v_lshlrev_b32 v4, 16, v48\n\t"
      "v_and_b32 v5, s50, v48\n\t"
      "v_pk_mul_f32 v[4:5], v[4:5], s[48:49]\n\t"
      "v_cvt_pk_bf16_f32 v48, v4, v5\n\t"
      "v_lshlrev_b32 v6, 16, v49\n\t"
      "v_and_b32 v7, s50, v49\n\t"
      "v_pk_mul_f32 v[6:7], v[6:7], s[48:49]\n\t"
      "v_cvt_pk_bf16_f32 v49, v6, v7\n\t"
      "v_lshlrev_b32 v24, 16, v50\n\t"
      "v_and_b32 v25, s50, v50\n\t"
      "v_pk_mul_f32 v[24:25], v[24:25], s[48:49]\n\t"
      "v_cvt_pk_bf16_f32 v50, v24, v25\n\t"
      "v_lshlrev_b32 v26, 16, v51\n\t"
      "v_and_b32 v27, s50, v51\n\t"
      "v_pk_mul_f32 v[26:27], v[26:27], s[48:49]\n\t"
      "v_cvt_pk_bf16_f32 v51, v26, v27\n\t"
      "s_waitcnt lgkmcnt(2)\n\t"
      "v_lshlrev_b32 v4, 16, v52\n\t"
      "v_and_b32 v5, s50, v52\n\t"
      "v_pk_mul_f32 v[4:5], v[4:5], s[48:49]\n\t"
      "v_cvt_pk_bf16_f32 v52, v4, v5\n\t"
      "v_lshlrev_b32 v6, 16, v53\n\t"
      "v_and_b32 v7, s50, v53\n\t"
      "v_pk_mul_f32 v[6:7], v[6:7], s[48:49]\n\t"
      "v_cvt_pk_bf16_f32 v53, v6, v7\n\t"
      "v_lshlrev_b32 v24, 16, v54\n\t"
      "v_and_b32 v25, s50, v54\n\t"
      "v_pk_mul_f32 v[24:25], v[24:25], s[48:49]\n\t"
      "v_cvt_pk_bf16_f32 v54, v24, v25\n\t"
      "v_lshlrev_b32 v26, 16, v55\n\t"
      "v_and_b32 v27, s50, v55\n\t"
      "v_pk_mul_f32 v[26:27], v[26:27], s[48:49]\n\t"
      "v_cvt_pk_bf16_f32 v55, v26, v27\n\t"
      "s_waitcnt lgkmcnt(1)\n\t"
      "v_lshlrev_b32 v4, 16, v56\n\t"
      "v_and_b32 v5, s50, v56\n\t"
      "v_pk_mul_f32 v[4:5], v[4:5], s[48:49]\n\t"
      "v_cvt_pk_bf16_f32 v56, v4, v5\n\t"
      "v_lshlrev_b32 v6, 16, v57\n\t"
      "v_and_b32 v7, s50, v57\n\t"
      "v_pk_mul_f32 v[6:7], v[6:7], s[48:49]\n\t"
      "v_cvt_pk_bf16_f32 v57, v6, v7\n\t"
      "v_lshlrev_b32 v24, 16, v58\n\t"
      "v_and_b32 v25, s50, v58\n\t"
      "v_pk_mul_f32 v[24:25], v[24:25], s[48:49]\n\t"
      "v_cvt_pk_bf16_f32 v58, v24, v25\n\t"
      "v_lshlrev_b32 v26, 16, v59\n\t"
      "v_and_b32 v27, s50, v59\n\t"
      "v_pk_mul_f32 v[26:27], v[26:27], s[48:49]\n\t"
      "v_cvt_pk_bf16_f32 v59, v26, v27\n\t"
      "s_waitcnt lgkmcnt(0)\n\t"
      "v_lshlrev_b32 v4, 16, v60\n\t"
      "v_and_b32 v5, s50, v60\n\t"
      "v_pk_mul_f32 v[4:5], v[4:5], s[48:49]\n\t"
      "v_cvt_pk_bf16_f32 v60, v4, v5\n\t"
      "v_lshlrev_b32 v6, 16, v61\n\t"
      "v_and_b32 v7, s50, v61\n\t"
      "v_pk_mul_f32 v[6:7], v[6:7], s[48:49]\n\t"
      "v_cvt_pk_bf16_f32 v61, v6, v7\n\t"
      "v_lshlrev_b32 v24, 16, v62\n\t"
      "v_and_b32 v25, s50, v62\n\t"
      "v_pk_mul_f32 v[24:25], v[24:25], s[48:49]\n\t"
      "v_cvt_pk_bf16_f32 v62, v24, v25\n\t"
      "v_lshlrev_b32 v26, 16, v63\n\t"
      "v_and_b32 v27, s50, v63\n\t"
      "v_pk_mul_f32 v[26:27], v[26:27], s[48:49]\n\t"
      "v_cvt_pk_bf16_f32 v63, v26, v27\n\t"
      "s_nop 1\n\t"
      "v_mfma_f32_16x16x32_bf16 v[112:115], a[32:35], v[48:51], 0\n\t"
      "v_mfma_f32_16x16x32_bf16 v[112:115], a[36:39], v[52:55], v[112:115]\n\t"
      "v_mfma_f32_16x16x32_bf16 v[112:115], a[40:43], v[56:59], v[112:115]\n\t"
      "v_mfma_f32_16x16x32_bf16 v[112:115], a[44:47], v[60:63], v[112:115]\n\t"
      "v_mfma_f32_16x16x32_bf16 v[116:119], a[48:51], v[48:51], 0\n\t"
      "v_mfma_f32_16x16x32_bf16 v[116:119], a[52:55], v[52:55], v[116:119]\n\t"
      "v_mfma_f32_16x16x32_bf16 v[116:119], a[56:59], v[56:59], v[116:119]\n\t"
      "v_mfma_f32_16x16x32_bf16 v[116:119], a[60:63], v[60:63], v[116:119]\n\t"
      "s_add_u32 m0, s37, 24576\n\t"
      "s_nop 0\n\t"
      "buffer_load_dwordx4 v32, s[24:27], s39 offen nt lds\n\t"
      "buffer_load_dwordx4 v33, s[24:27], s39 offen offset:1024 nt lds\n\t"
      "s_add_u32 s39, s39, 8192\n\t"
      "s_nop 3\n\t"
      "v_max3_f32 v4, v112, v113, v114\n\t"
      "v_max3_f32 v4, v4, v115, v116\n\t"
      "v_max3_f32 v4, v4, v117, v118\n\t"
      "v_max_f32 v4, v4, v119\n\t"
      "v_mov_b32 v5, v4\n\t"
      "s_nop 1\n\t"
      "v_permlane16_swap_b32 v4, v5\n\t"
      "s_nop 1\n\t"
      "v_max_f32 v4, v4, v5\n\t"
      "v_mov_b32 v5, v4\n\t"
      "s_nop 1\n\t"
      "v_permlane32_swap_b32 v4, v5\n\t"
      "s_nop 1\n\t"
      "v_max_f32 v4, v4, v5\n\t"
      "v_mul_f32 v88, -1.0, v4\n\t"
      "v_mul_f32 v89, -1.0, v4\n\t"
      "v_mul_f32 v90, -1.0, v4\n\t"
      "v_mul_f32 v91, -1.0, v4\n\t"
      "v_sub_f32 v112, v112, v4\n\t"
      "v_sub_f32 v113, v113, v4\n\t"
      "v_sub_f32 v114, v114, v4\n\t"
      "v_sub_f32 v115, v115, v4\n\t"
      "v_sub_f32 v116, v116, v4\n\t"
      "v_sub_f32 v117, v117, v4\n\t"
      "v_sub_f32 v118, v118, v4\n\t"
      "v_sub_f32 v119, v119, v4\n\t"
      "v_exp_f32 v112, v112\n\t"
      "v_exp_f32 v113, v113\n\t"
      "s_nop 0\n\t"
      "v_add_f32 v30, v30, v112\n\t"
      "v_add_f32 v30, v30, v113\n\t"
      "v_cvt_pk_bf16_f32 v112, v112, v113\n\t"
      "v_exp_f32 v114, v114\n\t"
      "v_exp_f32 v115, v115\n\t"
      "s_nop 0\n\t"
      "v_add_f32 v30, v30, v114\n\t"
      "s_waitcnt vmcnt(11)\n\t"
      "ds_read_b128 v[64:67], v20 offset:12288\n\t"
      "ds_read_b128 v[68:71], v21 offset:12288\n\t"
      "ds_read_b128 v[72:75], v22 offset:12288\n\t"
      "ds_read_b128 v[76:79], v23 offset:12288\n\t"
      "s_waitcnt lgkmcnt(3)\n\t"
      "v_lshlrev_b32 v4, 16, v64\n\t"
      "v_and_b32 v5, s50, v64\n\t"
      "v_pk_mul_f32 v[4:5], v[4:5], s[48:49]\n\t"
      "v_cvt_pk_bf16_f32 v64, v4, v5\n\t"
      "v_lshlrev_b32 v6, 16, v65\n\t"
      "v_and_b32 v7, s50, v65\n\t"
      "v_pk_mul_f32 v[6:7], v[6:7], s[48:49]\n\t"
      "v_cvt_pk_bf16_f32 v65, v6, v7\n\t"
      "v_lshlrev_b32 v24, 16, v66\n\t"
      "v_and_b32 v25, s50, v66\n\t"
      "v_pk_mul_f32 v[24:25], v[24:25], s[48:49]\n\t"
      "v_cvt_pk_bf16_f32 v66, v24, v25\n\t"
      "v_lshlrev_b32 v26, 16, v67\n\t"
      "v_and_b32 v27, s50, v67\n\t"
      "v_pk_mul_f32 v[26:27], v[26:27], s[48:49]\n\t"
      "v_cvt_pk_bf16_f32 v67, v26, v27\n\t"
      "s_nop 1\n\t"
      "v_mfma_f32_16x16x32_bf16 v[120:123], a[32:35], v[64:67], 0\n\t"
      "v_mfma_f32_16x16x32_bf16 v[124:127], a[48:51], v[64:67], 0\n\t"
      "s_waitcnt lgkmcnt(2)\n\t"
      "v_lshlrev_b32 v4, 16, v68\n\t"
      "v_and_b32 v5, s50, v68\n\t"
      "v_pk_mul_f32 v[4:5], v[4:5], s[48:49]\n\t"
      "v_cvt_pk_bf16_f32 v68, v4, v5\n\t"
      "v_lshlrev_b32 v6, 16, v69\n\t"
      "v_and_b32 v7, s50, v69\n\t"
      "v_pk_mul_f32 v[6:7], v[6:7], s[48:49]\n\t"
      "v_cvt_pk_bf16_f32 v69, v6, v7\n\t"
      "v_lshlrev_b32 v24, 16, v70\n\t"
      "v_and_b32 v25, s50, v70\n\t"
      "v_pk_mul_f32 v[24:25], v[24:25], s[48:49]\n\t"
      "v_cvt_pk_bf16_f32 v70, v24, v25\n\t"
      "v_lshlrev_b32 v26, 16, v71\n\t"
      "v_and_b32 v27, s50, v71\n\t"
      "v_pk_mul_f32 v[26:27], v[26:27], s[48:49]\n\t"
      "v_cvt_pk_bf16_f32 v71, v26, v27\n\t"
      "s_nop 1\n\t"
      "v_mfma_f32_16x16x32_bf16 v[120:123], a[36:39], v[68:71], v[120:123]\n\t"
      "v_mfma_f32_16x16x32_bf16 v[124:127], a[52:55], v[68:71], v[124:127]\n\t"
      "s_waitcnt lgkmcnt(1)\n\t"
      "v_lshlrev_b32 v4, 16, v72\n\t"
      "v_and_b32 v5, s50, v72\n\t"
      "v_pk_mul_f32 v[4:5], v[4:5], s[48:49]\n\t"
      "v_cvt_pk_bf16_f32 v72, v4, v5\n\t"
      "v_lshlrev_b32 v6, 16, v73\n\t"
      "v_and_b32 v7, s50, v73\n\t"
      "v_pk_mul_f32 v[6:7], v[6:7], s[48:49]\n\t"
      "v_cvt_pk_bf16_f32 v73, v6, v7\n\t"
      "v_lshlrev_b32 v24, 16, v74\n\t"
      "v_and_b32 v25, s50, v74\n\t"
      "v_pk_mul_f32 v[24:25], v[24:25], s[48:49]\n\t"
      "v_cvt_pk_bf16_f32 v74, v24, v25\n\t"
      "v_lshlrev_b32 v26, 16, v75\n\t"
      "v_and_b32 v27, s50, v75\n\t"
      "v_pk_mul_f32 v[26:27], v[26:27], s[48:49]\n\t"
      "v_cvt_pk_bf16_f32 v75, v26, v27\n\t"
      "s_nop 1\n\t"
      "v_mfma_f32_16x16x32_bf16 v[120:123], a[40:43], v[72:75], v[120:123]\n\t"
      "v_mfma_f32_16x16x32_bf16 v[124:127], a[56:59], v[72:75], v[124:127]\n\t"
      "s_waitcnt lgkmcnt(0)\n\t"
      "v_lshlrev_b32 v4, 16, v76\n\t"
      "v_and_b32 v5, s50, v76\n\t"
      "v_pk_mul_f32 v[4:5], v[4:5], s[48:49]\n\t"
      "v_cvt_pk_bf16_f32 v76, v4, v5\n\t"
      "v_lshlrev_b32 v6, 16, v77\n\t"
      "v_and_b32 v7, s50, v77\n\t"
      "v_pk_mul_f32 v[6:7], v[6:7], s[48:49]\n\t"
      "v_cvt_pk_bf16_f32 v77, v6, v7\n\t"
      "v_lshlrev_b32 v24, 16, v78\n\t"
      "v_and_b32 v25, s50, v78\n\t"
      "v_pk_mul_f32 v[24:25], v[24:25], s[48:49]\n\t"
      "v_cvt_pk_bf16_f32 v78, v24, v25\n\t"
      "v_lshlrev_b32 v26, 16, v79\n\t"
      "v_and_b32 v27, s50, v79\n\t"
      "v_pk_mul_f32 v[26:27], v[26:27], s[48:49]\n\t"
      "v_cvt_pk_bf16_f32 v79, v26, v27\n\t"
      "s_nop 1\n\t"
      "v_mfma_f32_16x16x32_bf16 v[120:123], a[44:47], v[76:79], v[120:123]\n\t"
      "v_mfma_f32_16x16x32_bf16 v[124:127], a[60:63], v[76:79], v[124:127]\n\t"
      "s_waitcnt vmcnt(9)\n\t"
      "s_barrier\n\t"
      "ds_read_b64_tr_b16 a[64:65], v36 offset:0\n\t"
      "ds_read_b64_tr_b16 a[66:67], v36 offset:4096\n\t"
      "ds_read_b64_tr_b16 a[68:69], v37 offset:0\n\t"
      "ds_read_b64_tr_b16 a[70:71], v37 offset:4096\n\t"
      "ds_read_b64_tr_b16 a[72:73], v38 offset:0\n\t"
      "ds_read_b64_tr_b16 a[74:75], v38 offset:4096\n\t"
      "ds_read_b64_tr_b16 a[76:77], v39 offset:0\n\t"
      "ds_read_b64_tr_b16 a[78:79], v39 offset:4096\n\t"
      "ds_read_b64_tr_b16 a[80:81], v40 offset:0\n\t"
      "ds_read_b64_tr_b16 a[82:83], v40 offset:4096\n\t"
      "ds_read_b64_tr_b16 a[84:85], v41 offset:0\n\t"
      "ds_read_b64_tr_b16 a[86:87], v41 offset:4096\n\t"
      "ds_read_b64_tr_b16 a[88:89], v42 offset:0\n\t"
      "ds_read_b64_tr_b16 a[90:91], v42 offset:4096\n\t"
      "ds_read_b64_tr_b16 a[92:93], v43 offset:0\n\t"
      "ds_read_b64_tr_b16 a[94:95], v43 offset:4096\n\t"
      "v_max3_f32 v6, v120, v121, v122\n\t"
      "v_max3_f32 v6, v6, v123, v124\n\t"
      "v_max3_f32 v6, v6, v125, v126\n\t"
      "v_max_f32 v6, v6, v127\n\t"
      "v_mov_b32 v7, v6\n\t"
      "s_nop 1\n\t"
      "v_permlane16_swap_b32 v6, v7\n\t"
      "s_nop 1\n\t"
      "v_max_f32 v6, v6, v7\n\t"
      "v_mov_b32 v7, v6\n\t"
      "s_nop 1\n\t"
      "v_permlane32_swap_b32 v6, v7\n\t"
      "s_nop 1\n\t"
      "v_max_f32 v6, v6, v7\n\t"
      "s_waitcnt lgkmcnt(0)\n\t"
      "v_mfma_f32_16x16x32_bf16 v[128:131], a[64:67], v[96:99], v[128:131]\n\t"
      "v_exp_f32 v116, v116\n\t"
      "v_mfma_f32_16x16x32_bf16 v[160:163], a[64:67], v[104:107], v[160:163]\n\t"
      "s_add_u32 m0, s38, 16384\n\t"
      "s_nop 0\n\t"
      "buffer_load_dwordx4 v34, s[28:31], s40 offen nt lds\n\t"
      "buffer_load_dwordx4 v35, s[28:31], s40 offen offset:1024 nt lds\n\t"
      "s_add_u32 s40, s40, 8192\n\t"
      "v_mul_f32 v92, -1.0, v6\n\t"
      "v_mul_f32 v93, -1.0, v6\n\t"
      "v_mul_f32 v94, -1.0, v6\n\t"
      "v_mul_f32 v95, -1.0, v6\n\t"
      "v_add_f32 v30, v30, v115\n\t"
      "v_cvt_pk_bf16_f32 v113, v114, v115\n\t"
      "v_mfma_f32_16x16x32_bf16 v[132:135], a[68:71], v[96:99], v[132:135]\n\t"
      "v_exp_f32 v117, v117\n\t"
      "v_mfma_f32_16x16x32_bf16 v[164:167], a[68:71], v[104:107], v[164:167]\n\t"
      "s_mov_b32 m0, 196608\n\t"
      "s_nop 0\n\t"
      "buffer_load_dwordx4 v32, s[60:63], s70 offen nt lds\n\t"
      "v_sub_f32 v120, v120, v6\n\t"
      "v_sub_f32 v121, v121, v6\n\t"
      "v_sub_f32 v122, v122, v6\n\t"
      "v_sub_f32 v123, v123, v6\n\t"
      "v_add_f32 v30, v30, v116\n\t"
      "v_mfma_f32_16x16x32_bf16 v[136:139], a[72:75], v[96:99], v[136:139]\n\t"
      "v_exp_f32 v118, v118\n\t"
      "v_mfma_f32_16x16x32_bf16 v[168:171], a[72:75], v[104:107], v[168:171]\n\t"
      "v_sub_f32 v124, v124, v6\n\t"
      "v_sub_f32 v125, v125, v6\n\t"
      "v_sub_f32 v126, v126, v6\n\t"
      "v_sub_f32 v127, v127, v6\n\t"
      "v_add_f32 v30, v30, v117\n\t"
      "v_cvt_pk_bf16_f32 v114, v116, v117\n\t"
      "v_mfma_f32_16x16x32_bf16 v[140:143], a[76:79], v[96:99], v[140:143]\n\t"
      "s_waitcnt vmcnt(10)\n\t"
      "s_barrier\n\t"
      "v_exp_f32 v119, v119\n\t"
      "v_mfma_f32_16x16x32_bf16 v[172:175], a[76:79], v[104:107], v[172:175]\n\t"
      "v_add_f32 v30, v30, v118\n\t"
      "v_mfma_f32_16x16x32_bf16 v[144:147], a[80:83], v[96:99], v[144:147]\n\t"
      "v_exp_f32 v120, v120\n\t"
      "v_mfma_f32_16x16x32_bf16 v[176:179], a[80:83], v[104:107], v[176:179]\n\t"
      "ds_read_b128 a[32:35], v44 offset:8192\n\t"
      "ds_read_b128 a[36:39], v45 offset:8192\n\t"
      "s_add_u32 m0, s37, 0\n\t"
      "s_nop 0\n\t"
      "buffer_load_dwordx4 v32, s[24:27], s39 offen nt lds\n\t"
      "buffer_load_dwordx4 v33, s[24:27], s39 offen offset:1024 nt lds\n\t"
      "s_add_u32 s39, s39, 8192\n\t"
      "v_add_f32 v30, v30, v119\n\t"
      "v_cvt_pk_bf16_f32 v115, v118, v119\n\t"
      "v_mfma_f32_16x16x32_bf16 v[148:151], a[84:87], v[96:99], v[148:151]\n\t"
      "v_exp_f32 v121, v121\n\t"
      "v_mfma_f32_16x16x32_bf16 v[180:183], a[84:87], v[104:107], v[180:183]\n\t"
      "ds_read_b128 a[40:43], v46 offset:8192\n\t"
      "ds_read_b128 a[44:47], v47 offset:8192\n\t"
      "s_add_u32 m0, s38, 24576\n\t"
      "s_nop 0\n\t"
      "buffer_load_dwordx4 v34, s[28:31], s40 offen nt lds\n\t"
      "buffer_load_dwordx4 v35, s[28:31], s40 offen offset:1024 nt lds\n\t"
      "s_add_u32 s40, s40, 8192\n\t"
      "v_add_f32 v31, v31, v120\n\t"
      "v_mfma_f32_16x16x32_bf16 v[152:155], a[88:91], v[96:99], v[152:155]\n\t"
      "v_exp_f32 v122, v122\n\t"
      "v_mfma_f32_16x16x32_bf16 v[184:187], a[88:91], v[104:107], v[184:187]\n\t"
      "ds_read_b128 a[48:51], v44 offset:12288\n\t"
      "ds_read_b128 a[52:55], v45 offset:12288\n\t"
      "s_mov_b32 m0, 196608\n\t"
      "s_nop 0\n\t"
      "buffer_load_dwordx4 v32, s[60:63], s70 offen nt lds\n\t"
      "v_add_f32 v31, v31, v121\n\t"
      "v_cvt_pk_bf16_f32 v120, v120, v121\n\t"
      "v_mfma_f32_16x16x32_bf16 v[156:159], a[92:95], v[96:99], v[156:159]\n\t"
      "v_exp_f32 v123, v123\n\t"
      "v_mfma_f32_16x16x32_bf16 v[188:191], a[92:95], v[104:107], v[188:191]\n\t"
      "ds_read_b128 a[56:59], v46 offset:12288\n\t"
      "ds_read_b128 a[60:63], v47 offset:12288\n\t"
      "v_add_f32 v31, v31, v122\n\t"
      "s_branch ENTRY%=\n\t"
      "LOOP%=:\n\t"
      "; ---- period u=0\n\t"
      "s_waitcnt lgkmcnt(4)\n\t"
      "v_mfma_f32_16x16x32_bf16 v[96:99], a[32:35], a[0:3], v[80:83]\n\t"
      "v_exp_f32 v124, v124\n\t"
      "v_mfma_f32_16x16x32_bf16 v[96:99], a[36:39], a[4:7], v[96:99]\n\t"
      "v_add_f32 v31, v31, v123\n\t"
      "v_cvt_pk_bf16_f32 v121, v122, v123\n\t"
      "v_mfma_f32_16x16x32_bf16 v[96:99], a[40:43], a[8:11], v[96:99]\n\t"
      "v_exp_f32 v125, v125\n\t"
      "v_mfma_f32_16x16x32_bf16 v[96:99], a[44:47], a[12:15], v[96:99]\n\t"
      "v_add_f32 v31, v31, v124\n\t"
      "s_waitcnt lgkmcnt(0)\n\t"
      "v_mfma_f32_16x16x32_bf16 v[100:103], a[48:51], a[0:3], v[80:83]\n\t"
      "s_add_u32 m0, s37, 0\n\t"
      "v_exp_f32 v126, v126\n\t"
      "v_mfma_f32_16x16x32_bf16 v[100:103], a[52:55], a[4:7], v[100:103]\n\t"
      "v_add_f32 v31, v31, v125\n\t"
      "v_cvt_pk_bf16_f32 v122, v124, v125\n\t"
      "v_mfma_f32_16x16x32_bf16 v[100:103], a[56:59], a[8:11], v[100:103]\n\t"
      "s_waitcnt vmcnt(11)\n\t"
      "s_barrier\n\t"
      "v_exp_f32 v127, v127\n\t"
      "v_mfma_f32_16x16x32_bf16 v[100:103], a[60:63], a[12:15], v[100:103]\n\t"
      "buffer_load_dwordx4 v32, s[24:27], s39 offen nt lds\n\t"
      "v_add_f32 v31, v31, v126\n\t"
      "v_mfma_f32_16x16x32_bf16 v[104:107], a[32:35], a[16:19], v[84:87]\n\t"
      "v_exp_f32 v96, v96\n\t"
      "v_mfma_f32_16x16x32_bf16 v[104:107], a[36:39], a[20:23], v[104:107]\n\t"
      "v_add_f32 v31, v31, v127\n\t"
      "v_cvt_pk_bf16_f32 v123, v126, v127\n\t"
      "v_mfma_f32_16x16x32_bf16 v[104:107], a[40:43], a[24:27], v[104:107]\n\t"
      "v_exp_f32 v97, v97\n\t"
      "v_mfma_f32_16x16x32_bf16 v[104:107], a[44:47], a[28:31], v[104:107]\n\t"
      "v_add_f32 v28, v28, v96\n\t"
      "v_mfma_f32_16x16x32_bf16 v[108:111], a[48:51], a[16:19], v[84:87]\n\t"
      "v_exp_f32 v98, v98\n\t"
      "v_mfma_f32_16x16x32_bf16 v[108:111], a[52:55], a[20:23], v[108:111]\n\t"
      "v_add_f32 v28, v28, v97\n\t"
      "v_cvt_pk_bf16_f32 v96, v96, v97\n\t"
      "v_mfma_f32_16x16x32_bf16 v[108:111], a[56:59], a[24:27], v[108:111]\n\t"
      "v_exp_f32 v99, v99\n\t"
      "v_mfma_f32_16x16x32_bf16 v[108:111], a[60:63], a[28:31], v[108:111]\n\t"
      "v_add_f32 v28, v28, v98\n\t"
      "v_mfma_f32_16x16x32_bf16 v[192:195], a[64:67], v[112:115], v[192:195]\n\t"
      "v_exp_f32 v100, v100\n\t"
      "v_mfma_f32_16x16x32_bf16 v[224:227], a[64:67], v[120:123], v[224:227]\n\t"
      "v_add_f32 v28, v28, v99\n\t"
      "v_cvt_pk_bf16_f32 v97, v98, v99\n\t"
      "v_mfma_f32_16x16x32_bf16 v[196:199], a[68:71], v[112:115], v[196:199]\n\t"
      "v_exp_f32 v101, v101\n\t"
      "v_mfma_f32_16x16x32_bf16 v[228:231], a[68:71], v[120:123], v[228:231]\n\t"
      "ds_read_b64_tr_b16 a[64:65], v36 offset:0\n\t"
      "buffer_load_dwordx4 v33, s[24:27], s39 offen offset:1024 nt lds\n\t"
      "s_add_u32 m0, s38, 24576\n\t"
      "v_add_f32 v28, v28, v100\n\t"
      "v_mfma_f32_16x16x32_bf16 v[200:203], a[72:75], v[112:115], v[200:203]\n\t"
      "v_exp_f32 v102, v102\n\t"
      "v_mfma_f32_16x16x32_bf16 v[232:235], a[72:75], v[120:123], v[232:235]\n\t"
      "ds_read_b64_tr_b16 a[66:67], v36 offset:4096\n\t"
      "v_add_f32 v28, v28, v101\n\t"
      "v_cvt_pk_bf16_f32 v98, v100, v101\n\t"
      "v_mfma_f32_16x16x32_bf16 v[204:207], a[76:79], v[112:115], v[204:207]\n\t"
      "v_exp_f32 v103, v103\n\t"
      "v_mfma_f32_16x16x32_bf16 v[236:239], a[76:79], v[120:123], v[236:239]\n\t"
      "ds_read_b64_tr_b16 a[68:69], v37 offset:0\n\t"
      "v_add_f32 v28, v28, v102\n\t"
      "s_add_u32 s39, s39, 8192\n\t"
      "v_mfma_f32_16x16x32_bf16 v[208:211], a[80:83], v[112:115], v[208:211]\n\t"
      "v_exp_f32 v104, v104\n\t"
      "v_mfma_f32_16x16x32_bf16 v[240:243], a[80:83], v[120:123], v[240:243]\n\t"
      "ds_read_b64_tr_b16 a[70:71], v37 offset:4096\n\t"
      "v_add_f32 v28, v28, v103\n\t"
      "v_cvt_pk_bf16_f32 v99, v102, v103\n\t"
      "v_mfma_f32_16x16x32_bf16 v[212:215], a[84:87], v[112:115], v[212:215]\n\t"
      "v_exp_f32 v105, v105\n\t"
      "v_mfma_f32_16x16x32_bf16 v[244:247], a[84:87], v[120:123], v[244:247]\n\t"
      "ds_read_b64_tr_b16 a[72:73], v38 offset:0\n\t"
      "v_add_f32 v29, v29, v104\n\t"
      "v_mfma_f32_16x16x32_bf16 v[216:219], a[88:91], v[112:115], v[216:219]\n\t"
      "v_exp_f32 v106, v106\n\t"
      "v_mfma_f32_16x16x32_bf16 v[248:251], a[88:91], v[120:123], v[248:251]\n\t"
      "ds_read_b64_tr_b16 a[74:75], v38 offset:4096\n\t"
      "v_add_f32 v29, v29, v105\n\t"
      "v_cvt_pk_bf16_f32 v104, v104, v105\n\t"
      "v_mfma_f32_16x16x32_bf16 v[220:223], a[92:95], v[112:115], v[220:223]\n\t"
      "v_exp_f32 v107, v107\n\t"
      "v_mfma_f32_16x16x32_bf16 v[252:255], a[92:95], v[120:123], v[252:255]\n\t"
      "ds_read_b64_tr_b16 a[76:77], v39 offset:0\n\t"
      "buffer_load_dwordx4 v34, s[28:31], s40 offen nt lds\n\t"
      "v_add_f32 v29, v29, v106\n\t"
      "v_mfma_f32_16x16x32_bf16 v[112:115], a[32:35], v[48:51], v[88:91]\n\t"
      "v_exp_f32 v108, v108\n\t"
      "v_mfma_f32_16x16x32_bf16 v[112:115], a[36:39], v[52:55], v[112:115]\n\t"
      "ds_read_b64_tr_b16 a[78:79], v39 offset:4096\n\t"
      "v_add_f32 v29, v29, v107\n\t"
      "v_cvt_pk_bf16_f32 v105, v106, v107\n\t"
      "v_mfma_f32_16x16x32_bf16 v[112:115], a[40:43], v[56:59], v[112:115]\n\t"
      "v_exp_f32 v109, v109\n\t"
      "v_mfma_f32_16x16x32_bf16 v[112:115], a[44:47], v[60:63], v[112:115]\n\t"
      "ds_read_b64_tr_b16 a[80:81], v40 offset:0\n\t"
      "v_add_f32 v29, v29, v108\n\t"
      "v_mfma_f32_16x16x32_bf16 v[116:119], a[48:51], v[48:51], v[88:91]\n\t"
      "v_exp_f32 v110, v110\n\t"
      "v_mfma_f32_16x16x32_bf16 v[116:119], a[52:55], v[52:55], v[116:119]\n\t"
      "ds_read_b64_tr_b16 a[82:83], v40 offset:4096\n\t"
      "v_add_f32 v29, v29, v109\n\t"
      "v_cvt_pk_bf16_f32 v106, v108, v109\n\t"
      "v_mfma_f32_16x16x32_bf16 v[116:119], a[56:59], v[56:59], v[116:119]\n\t"
      "v_exp_f32 v111, v111\n\t"
      "v_mfma_f32_16x16x32_bf16 v[116:119], a[60:63], v[60:63], v[116:119]\n\t"
      "ds_read_b64_tr_b16 a[84:85], v41 offset:0\n\t"
      "v_add_f32 v29, v29, v110\n\t"
      "v_mfma_f32_16x16x32_bf16 v[120:123], a[32:35], v[64:67], v[92:95]\n\t"
      "v_exp_f32 v112, v112\n\t"
      "v_mfma_f32_16x16x32_bf16 v[120:123], a[36:39], v[68:71], v[120:123]\n\t"
      "ds_read_b64_tr_b16 a[86:87], v41 offset:4096\n\t"
      "v_add_f32 v29, v29, v111\n\t"
      "v_cvt_pk_bf16_f32 v107, v110, v111\n\t"
      "v_mfma_f32_16x16x32_bf16 v[120:123], a[40:43], v[72:75], v[120:123]\n\t"
      "v_exp_f32 v113, v113\n\t"
      "v_mfma_f32_16x16x32_bf16 v[120:123], a[44:47], v[76:79], v[120:123]\n\t"
      "ds_read_b64_tr_b16 a[88:89], v42 offset:0\n\t"
      "buffer_load_dwordx4 v35, s[28:31], s40 offen offset:1024 nt lds\n\t"
      "s_mov_b32 m0, s68\n\t"
      "v_add_f32 v30, v30, v112\n\t"
      "v_mfma_f32_16x16x32_bf16 v[124:127], a[48:51], v[64:67], v[92:95]\n\t"
      "v_exp_f32 v114, v114\n\t"
      "v_mfma_f32_16x16x32_bf16 v[124:127], a[52:55], v[68:71], v[124:127]\n\t"
      "ds_read_b64_tr_b16 a[90:91], v42 offset:4096\n\t"
      "v_add_f32 v30, v30, v113\n\t"
      "v_cvt_pk_bf16_f32 v112, v112, v113\n\t"
      "v_mfma_f32_16x16x32_bf16 v[124:127], a[56:59], v[72:75], v[124:127]\n\t"
      "v_exp_f32 v115, v115\n\t"
      "v_mfma_f32_16x16x32_bf16 v[124:127], a[60:63], v[76:79], v[124:127]\n\t"
      "ds_read_b64_tr_b16 a[92:93], v43 offset:0\n\t"
      "v_add_f32 v30, v30, v114\n\t"
      "s_add_u32 s40, s40, 8192\n\t"
      "s_waitcnt lgkmcnt(7)\n\t"
      "v_mfma_f32_16x16x32_bf16 v[128:131], a[64:67], v[96:99], v[128:131]\n\t"
      "v_exp_f32 v116, v116\n\t"
      "v_mfma_f32_16x16x32_bf16 v[160:163], a[64:67], v[104:107], v[160:163]\n\t"
      "ds_read_b64_tr_b16 a[94:95], v43 offset:4096\n\t"
      "ds_read_b128 a[32:35], v44 offset:8192\n\t"
      "v_add_f32 v30, v30, v115\n\t"
      "v_cvt_pk_bf16_f32 v113, v114, v115\n\t"
      "v_mfma_f32_16x16x32_bf16 v[132:135], a[68:71], v[96:99], v[132:135]\n\t"
      "v_exp_f32 v117, v117\n\t"
      "v_mfma_f32_16x16x32_bf16 v[164:167], a[68:71], v[104:107], v[164:167]\n\t"
      "ds_read_b128 a[36:39], v45 offset:8192\n\t"
      "v_add_f32 v30, v30, v116\n\t"
      "v_mfma_f32_16x16x32_bf16 v[136:139], a[72:75], v[96:99], v[136:139]\n\t"
      "v_exp_f32 v118, v118\n\t"
      "v_mfma_f32_16x16x32_bf16 v[168:171], a[72:75], v[104:107], v[168:171]\n\t"
      "ds_read_b128 a[40:43], v46 offset:8192\n\t"
      "v_add_f32 v30, v30, v117\n\t"
      "v_cvt_pk_bf16_f32 v114, v116, v117\n\t"
      "v_mfma_f32_16x16x32_bf16 v[140:143], a[76:79], v[96:99], v[140:143]\n\t"
      "v_exp_f32 v119, v119\n\t"
      "v_mfma_f32_16x16x32_bf16 v[172:175], a[76:79], v[104:107], v[172:175]\n\t"
      "ds_read_b128 a[44:47], v47 offset:8192\n\t"
      "buffer_load_dwordx4 v32, s[60:63], s70 offen nt lds\n\t"
      "v_add_f32 v30, v30, v118\n\t"
      "s_waitcnt lgkmcnt(4)\n\t"
      "v_mfma_f32_16x16x32_bf16 v[144:147], a[80:83], v[96:99], v[144:147]\n\t"
      "v_exp_f32 v120, v120\n\t"
      "v_mfma_f32_16x16x32_bf16 v[176:179], a[80:83], v[104:107], v[176:179]\n\t"
      "ds_read_b128 a[48:51], v44 offset:12288\n\t"
      "v_add_f32 v30, v30, v119\n\t"
      "v_cvt_pk_bf16_f32 v115, v118, v119\n\t"
      "v_mfma_f32_16x16x32_bf16 v[148:151], a[84:87], v[96:99], v[148:151]\n\t"
      "v_exp_f32 v121, v121\n\t"
      "v_mfma_f32_16x16x32_bf16 v[180:183], a[84:87], v[104:107], v[180:183]\n\t"
      "ds_read_b128 a[52:55], v45 offset:12288\n\t"
      "v_add_f32 v31, v31, v120\n\t"
      "v_mfma_f32_16x16x32_bf16 v[152:155], a[88:91], v[96:99], v[152:155]\n\t"
      "v_exp_f32 v122, v122\n\t"
      "v_mfma_f32_16x16x32_bf16 v[184:187], a[88:91], v[104:107], v[184:187]\n\t"
      "ds_read_b128 a[56:59], v46 offset:12288\n\t"
      "v_add_f32 v31, v31, v121\n\t"
      "v_cvt_pk_bf16_f32 v120, v120, v121\n\t"
      "v_mfma_f32_16x16x32_bf16 v[156:159], a[92:95], v[96:99], v[156:159]\n\t"
      "v_exp_f32 v123, v123\n\t"
      "v_mfma_f32_16x16x32_bf16 v[188:191], a[92:95], v[104:107], v[188:191]\n\t"
      "ds_read_b128 a[60:63], v47 offset:12288\n\t"
      "v_add_f32 v31, v31, v122\n\t"
      "; ---- period u=1\n\t"
      "ENTRY%=:\n\t"
      "s_waitcnt lgkmcnt(4)\n\t"
      "v_mfma_f32_16x16x32_bf16 v[96:99], a[32:35], a[0:3], v[80:83]\n\t"
      "v_exp_f32 v124, v124\n\t"
      "v_mfma_f32_16x16x32_bf16 v[96:99], a[36:39], a[4:7], v[96:99]\n\t"
      "v_add_f32 v31, v31, v123\n\t"
      "v_cvt_pk_bf16_f32 v121, v122, v123\n\t"
      "v_mfma_f32_16x16x32_bf16 v[96:99], a[40:43], a[8:11], v[96:99]\n\t"
      "v_exp_f32 v125, v125\n\t"
      "v_mfma_f32_16x16x32_bf16 v[96:99], a[44:47], a[12:15], v[96:99]\n\t"
      "v_add_f32 v31, v31, v124\n\t"
      "s_waitcnt lgkmcnt(0)\n\t"
      "v_mfma_f32_16x16x32_bf16 v[100:103], a[48:51], a[0:3], v[80:83]\n\t"
      "s_add_u32 m0, s37, 8192\n\t"
      "v_exp_f32 v126, v126\n\t"
      "v_mfma_f32_16x16x32_bf16 v[100:103], a[52:55], a[4:7], v[100:103]\n\t"
      "v_add_f32 v31, v31, v125\n\t"
      "v_cvt_pk_bf16_f32 v122, v124, v125\n\t"
      "v_mfma_f32_16x16x32_bf16 v[100:103], a[56:59], a[8:11], v[100:103]\n\t"
      "s_waitcnt vmcnt(11)\n\t"
      "s_barrier\n\t"
      "v_exp_f32 v127, v127\n\t"
      "v_mfma_f32_16x16x32_bf16 v[100:103], a[60:63], a[12:15], v[100:103]\n\t"
      "buffer_load_dwordx4 v32, s[24:27], s39 offen nt lds\n\t"
      "v_add_f32 v31, v31, v126\n\t"
      "v_mfma_f32_16x16x32_bf16 v[104:107], a[32:35], a[16:19], v[84:87]\n\t"
      "v_exp_f32 v96, v96\n\t"
      "v_mfma_f32_16x16x32_bf16 v[104:107], a[36:39], a[20:23], v[104:107]\n\t"
      "v_add_f32 v31, v31, v127\n\t"
      "v_cvt_pk_bf16_f32 v123, v126, v127\n\t"
      "v_mfma_f32_16x16x32_bf16 v[104:107], a[40:43], a[24:27], v[104:107]\n\t"
      "v_exp_f32 v97, v97\n\t"
      "v_mfma_f32_16x16x32_bf16 v[104:107], a[44:47], a[28:31], v[104:107]\n\t"
      "v_add_f32 v28, v28, v96\n\t"
      "v_mfma_f32_16x16x32_bf16 v[108:111], a[48:51], a[16:19], v[84:87]\n\t"
      "v_exp_f32 v98, v98\n\t"
      "v_mfma_f32_16x16x32_bf16 v[108:111], a[52:55], a[20:23], v[108:111]\n\t"
      "v_add_f32 v28, v28, v97\n\t"
      "v_cvt_pk_bf16_f32 v96, v96, v97\n\t"
      "v_mfma_f32_16x16x32_bf16 v[108:111], a[56:59], a[24:27], v[108:111]\n\t"
      "v_exp_f32 v99, v99\n\t"
      "v_mfma_f32_16x16x32_bf16 v[108:111], a[60:63], a[28:31], v[108:111]\n\t"
      "v_add_f32 v28, v28, v98\n\t"
      "v_mfma_f32_16x16x32_bf16 v[192:195], a[64:67], v[112:115], v[192:195]\n\t"
      "v_exp_f32 v100, v100\n\t"
      "v_mfma_f32_16x16x32_bf16 v[224:227], a[64:67], v[120:123], v[224:227]\n\t"
      "v_add_f32 v28, v28, v99\n\t"
      "v_cvt_pk_bf16_f32 v97, v98, v99\n\t"
      "v_mfma_f32_16x16x32_bf16 v[196:199], a[68:71], v[112:115], v[196:199]\n\t"
      "v_exp_f32 v101, v101\n\t"
      "v_mfma_f32_16x16x32_bf16 v[228:231], a[68:71], v[120:123], v[228:231]\n\t"
      "ds_read_b64_tr_b16 a[64:65], v36 offset:8192\n\t"
      "buffer_load_dwordx4 v33, s[24:27], s39 offen offset:1024 nt lds\n\t"
      "s_add_u32 m0, s38, 0\n\t"
      "v_add_f32 v28, v28, v100\n\t"
      "v_mfma_f32_16x16x32_bf16 v[200:203], a[72:75], v[112:115], v[200:203]\n\t"
      "v_exp_f32 v102, v102\n\t"
      "v_mfma_f32_16x16x32_bf16 v[232:235], a[72:75], v[120:123], v[232:235]\n\t"
      "ds_read_b64_tr_b16 a[66:67], v36 offset:12288\n\t"
      "v_add_f32 v28, v28, v101\n\t"
      "v_cvt_pk_bf16_f32 v98, v100, v101\n\t"
      "v_mfma_f32_16x16x32_bf16 v[204:207], a[76:79], v[112:115], v[204:207]\n\t"
      "v_exp_f32 v103, v103\n\t"
      "v_mfma_f32_16x16x32_bf16 v[236:239], a[76:79], v[120:123], v[236:239]\n\t"
      "ds_read_b64_tr_b16 a[68:69], v37 offset:8192\n\t"
      "v_add_f32 v28, v28, v102\n\t"
      "s_add_u32 s39, s39, 8192\n\t"
      "v_mfma_f32_16x16x32_bf16 v[208:211], a[80:83], v[112:115], v[208:211]\n\t"
      "v_exp_f32 v104, v104\n\t"
      "v_mfma_f32_16x16x32_bf16 v[240:243], a[80:83], v[120:123], v[240:243]\n\t"
      "ds_read_b64_tr_b16 a[70:71], v37 offset:12288\n\t"
      "v_add_f32 v28, v28, v103\n\t"
      "v_cvt_pk_bf16_f32 v99, v102, v103\n\t"
      "v_mfma_f32_16x16x32_bf16 v[212:215], a[84:87], v[112:115], v[212:215]\n\t"
      "v_exp_f32 v105, v105\n\t"
      "v_mfma_f32_16x16x32_bf16 v[244:247], a[84:87], v[120:123], v[244:247]\n\t"
      "ds_read_b64_tr_b16 a[72:73], v38 offset:8192\n\t"
      "v_add_f32 v29, v29, v104\n\t"
      "v_mfma_f32_16x16x32_bf16 v[216:219], a[88:91], v[112:115], v[216:219]\n\t"
      "v_exp_f32 v106, v106\n\t"
      "v_mfma_f32_16x16x32_bf16 v[248:251], a[88:91], v[120:123], v[248:251]\n\t"
      "ds_read_b64_tr_b16 a[74:75], v38 offset:12288\n\t"
      "v_add_f32 v29, v29, v105\n\t"
      "v_cvt_pk_bf16_f32 v104, v104, v105\n\t"
      "v_mfma_f32_16x16x32_bf16 v[220:223], a[92:95], v[112:115], v[220:223]\n\t"
      "v_exp_f32 v107, v107\n\t"
      "v_mfma_f32_16x16x32_bf16 v[252:255], a[92:95], v[120:123], v[252:255]\n\t"
      "ds_read_b64_tr_b16 a[76:77], v39 offset:8192\n\t"
      "buffer_load_dwordx4 v34, s[28:31], s40 offen nt lds\n\t"
      "v_add_f32 v29, v29, v106\n\t"
      "v_mfma_f32_16x16x32_bf16 v[112:115], a[32:35], v[48:51], v[88:91]\n\t"
      "v_exp_f32 v108, v108\n\t"
      "v_mfma_f32_16x16x32_bf16 v[112:115], a[36:39], v[52:55], v[112:115]\n\t"
      "ds_read_b64_tr_b16 a[78:79], v39 offset:12288\n\t"
      "v_add_f32 v29, v29, v107\n\t"
      "v_cvt_pk_bf16_f32 v105, v106, v107\n\t"
      "v_mfma_f32_16x16x32_bf16 v[112:115], a[40:43], v[56:59], v[112:115]\n\t"
      "v_exp_f32 v109, v109\n\t"
      "v_mfma_f32_16x16x32_bf16 v[112:115], a[44:47], v[60:63], v[112:115]\n\t"
      "ds_read_b64_tr_b16 a[80:81], v40 offset:8192\n\t"
      "v_add_f32 v29, v29, v108\n\t"
      "v_mfma_f32_16x16x32_bf16 v[116:119], a[48:51], v[48:51], v[88:91]\n\t"
      "v_exp_f32 v110, v110\n\t"
      "v_mfma_f32_16x16x32_bf16 v[116:119], a[52:55], v[52:55], v[116:119]\n\t"
      "ds_read_b64_tr_b16 a[82:83], v40 offset:12288\n\t"
      "v_add_f32 v29, v29, v109\n\t"
      "v_cvt_pk_bf16_f32 v106, v108, v109\n\t"
      "v_mfma_f32_16x16x32_bf16 v[116:119], a[56:59], v[56:59], v[116:119]\n\t"
      "v_exp_f32 v111, v111\n\t"
      "v_mfma_f32_16x16x32_bf16 v[116:119], a[60:63], v[60:63], v[116:119]\n\t"
      "ds_read_b64_tr_b16 a[84:85], v41 offset:8192\n\t"
      "v_add_f32 v29, v29, v110\n\t"
      "v_mfma_f32_16x16x32_bf16 v[120:123], a[32:35], v[64:67], v[92:95]\n\t"
      "v_exp_f32 v112, v112\n\t"
      "v_mfma_f32_16x16x32_bf16 v[120:123], a[36:39], v[68:71], v[120:123]\n\t"
      "ds_read_b64_tr_b16 a[86:87], v41 offset:12288\n\t"
      "v_add_f32 v29, v29, v111\n\t"
      "v_cvt_pk_bf16_f32 v107, v110, v111\n\t"
      "v_mfma_f32_16x16x32_bf16 v[120:123], a[40:43], v[72:75], v[120:123]\n\t"
      "v_exp_f32 v113, v113\n\t"
      "v_mfma_f32_16x16x32_bf16 v[120:123], a[44:47], v[76:79], v[120:123]\n\t"
      "ds_read_b64_tr_b16 a[88:89], v42 offset:8192\n\t"
      "buffer_load_dwordx4 v35, s[28:31], s40 offen offset:1024 nt lds\n\t"
      "s_mov_b32 m0, s68\n\t"
      "v_add_f32 v30, v30, v112\n\t"
      "v_mfma_f32_16x16x32_bf16 v[124:127], a[48:51], v[64:67], v[92:95]\n\t"
      "v_exp_f32 v114, v114\n\t"
      "v_mfma_f32_16x16x32_bf16 v[124:127], a[52:55], v[68:71], v[124:127]\n\t"
      "ds_read_b64_tr_b16 a[90:91], v42 offset:12288\n\t"
      "v_add_f32 v30, v30, v113\n\t"
      "v_cvt_pk_bf16_f32 v112, v112, v113\n\t"
      "v_mfma_f32_16x16x32_bf16 v[124:127], a[56:59], v[72:75], v[124:127]\n\t"
      "v_exp_f32 v115, v115\n\t"
      "v_mfma_f32_16x16x32_bf16 v[124:127], a[60:63], v[76:79], v[124:127]\n\t"
      "ds_read_b64_tr_b16 a[92:93], v43 offset:8192\n\t"
      "v_add_f32 v30, v30, v114\n\t"
      "s_add_u32 s40, s40, 8192\n\t"
      "s_waitcnt lgkmcnt(7)\n\t"
      "v_mfma_f32_16x16x32_bf16 v[128:131], a[64:67], v[96:99], v[128:131]\n\t"
      "v_exp_f32 v116, v116\n\t"
      "v_mfma_f32_16x16x32_bf16 v[160:163], a[64:67], v[104:107], v[160:163]\n\t"
      "ds_read_b64_tr_b16 a[94:95], v43 offset:12288\n\t"
      "ds_read_b128 a[32:35], v44 offset:16384\n\t"
      "v_add_f32 v30, v30, v115\n\t"
      "v_cvt_pk_bf16_f32 v113, v114, v115\n\t"
      "v_mfma_f32_16x16x32_bf16 v[132:135], a[68:71], v[96:99], v[132:135]\n\t"
      "v_exp_f32 v117, v117\n\t"
      "v_mfma_f32_16x16x32_bf16 v[164:167], a[68:71], v[104:107], v[164:167]\n\t"
      "ds_read_b128 a[36:39], v45 offset:16384\n\t"
      "v_add_f32 v30, v30, v116\n\t"
      "v_mfma_f32_16x16x32_bf16 v[136:139], a[72:75], v[96:99], v[136:139]\n\t"
      "v_exp_f32 v118, v118\n\t"
      "v_mfma_f32_16x16x32_bf16 v[168:171], a[72:75], v[104:107], v[168:171]\n\t"
      "ds_read_b128 a[40:43], v46 offset:16384\n\t"
      "v_add_f32 v30, v30, v117\n\t"
      "v_cvt_pk_bf16_f32 v114, v116, v117\n\t"
      "v_mfma_f32_16x16x32_bf16 v[140:143], a[76:79], v[96:99], v[140:143]\n\t"
      "v_exp_f32 v119, v119\n\t"
      "v_mfma_f32_16x16x32_bf16 v[172:175], a[76:79], v[104:107], v[172:175]\n\t"
      "ds_read_b128 a[44:47], v47 offset:16384\n\t"
      "buffer_load_dwordx4 v33, s[60:63], s70 offen offset:1024 nt lds\n\t"
      "v_add_f32 v30, v30, v118\n\t"
      "s_waitcnt lgkmcnt(4)\n\t"
      "v_mfma_f32_16x16x32_bf16 v[144:147], a[80:83], v[96:99], v[144:147]\n\t"
      "v_exp_f32 v120, v120\n\t"
      "v_mfma_f32_16x16x32_bf16 v[176:179], a[80:83], v[104:107], v[176:179]\n\t"
      "ds_read_b128 a[48:51], v44 offset:20480\n\t"
      "v_add_f32 v30, v30, v119\n\t"
      "v_cvt_pk_bf16_f32 v115, v118, v119\n\t"
      "v_mfma_f32_16x16x32_bf16 v[148:151], a[84:87], v[96:99], v[148:151]\n\t"
      "v_exp_f32 v121, v121\n\t"
      "v_mfma_f32_16x16x32_bf16 v[180:183], a[84:87], v[104:107], v[180:183]\n\t"
      "ds_read_b128 a[52:55], v45 offset:20480\n\t"
      "v_add_f32 v31, v31, v120\n\t"
      "v_mfma_f32_16x16x32_bf16 v[152:155], a[88:91], v[96:99], v[152:155]\n\t"
      "v_exp_f32 v122, v122\n\t"
      "v_mfma_f32_16x16x32_bf16 v[184:187], a[88:91], v[104:107], v[184:187]\n\t"
      "ds_read_b128 a[56:59], v46 offset:20480\n\t"
      "v_add_f32 v31, v31, v121\n\t"
      "v_cvt_pk_bf16_f32 v120, v120, v121\n\t"
      "v_mfma_f32_16x16x32_bf16 v[156:159], a[92:95], v[96:99], v[156:159]\n\t"
      "v_exp_f32 v123, v123\n\t"
      "v_mfma_f32_16x16x32_bf16 v[188:191], a[92:95], v[104:107], v[188:191]\n\t"
      "ds_read_b128 a[60:63], v47 offset:20480\n\t"
      "v_add_f32 v31, v31, v122\n\t"
      "; ---- period u=2\n\t"
      "s_waitcnt lgkmcnt(4)\n\t"
      "v_mfma_f32_16x16x32_bf16 v[96:99], a[32:35], a[0:3], v[80:83]\n\t"
      "v_exp_f32 v124, v124\n\t"
      "v_mfma_f32_16x16x32_bf16 v[96:99], a[36:39], a[4:7], v[96:99]\n\t"
      "v_add_f32 v31, v31, v123\n\t"
      "v_cvt_pk_bf16_f32 v121, v122, v123\n\t"
      "v_mfma_f32_16x16x32_bf16 v[96:99], a[40:43], a[8:11], v[96:99]\n\t"
      "v_exp_f32 v125, v125\n\t"
      "v_mfma_f32_16x16x32_bf16 v[96:99], a[44:47], a[12:15], v[96:99]\n\t"
      "v_add_f32 v31, v31, v124\n\t"
      "s_waitcnt lgkmcnt(0)\n\t"
      "v_mfma_f32_16x16x32_bf16 v[100:103], a[48:51], a[0:3], v[80:83]\n\t"
      "s_cmp_eq_u32 s41, 2\n\t"
      "s_cselect_b32 s26, 0, s26\n\t"
      "s_add_u32 m0, s37, 16384\n\t"
      "v_exp_f32 v126, v126\n\t"
      "v_mfma_f32_16x16x32_bf16 v[100:103], a[52:55], a[4:7], v[100:103]\n\t"
      "v_add_f32 v31, v31, v125\n\t"
      "v_cvt_pk_bf16_f32 v122, v124, v125\n\t"
      "v_mfma_f32_16x16x32_bf16 v[100:103], a[56:59], a[8:11], v[100:103]\n\t"
      "s_waitcnt vmcnt(11)\n\t"
      "s_barrier\n\t"
      "v_exp_f32 v127, v127\n\t"
      "v_mfma_f32_16x16x32_bf16 v[100:103], a[60:63], a[12:15], v[100:103]\n\t"
      "buffer_load_dwordx4 v32, s[24:27], s39 offen nt lds\n\t"
      "v_add_f32 v31, v31, v126\n\t"
      "v_mfma_f32_16x16x32_bf16 v[104:107], a[32:35], a[16:19], v[84:87]\n\t"
      "v_exp_f32 v96, v96\n\t"
      "v_mfma_f32_16x16x32_bf16 v[104:107], a[36:39], a[20:23], v[104:107]\n\t"
      "v_add_f32 v31, v31, v127\n\t"
      "v_cvt_pk_bf16_f32 v123, v126, v127\n\t"
      "v_mfma_f32_16x16x32_bf16 v[104:107], a[40:43], a[24:27], v[104:107]\n\t"
      "v_exp_f32 v97, v97\n\t"
      "v_mfma_f32_16x16x32_bf16 v[104:107], a[44:47], a[28:31], v[104:107]\n\t"
      "v_add_f32 v28, v28, v96\n\t"
      "v_mfma_f32_16x16x32_bf16 v[108:111], a[48:51], a[16:19], v[84:87]\n\t"
      "v_exp_f32 v98, v98\n\t"
      "v_mfma_f32_16x16x32_bf16 v[108:111], a[52:55], a[20:23], v[108:111]\n\t"
      "v_add_f32 v28, v28, v97\n\t"
      "v_cvt_pk_bf16_f32 v96, v96, v97\n\t"
      "v_mfma_f32_16x16x32_bf16 v[108:111], a[56:59], a[24:27], v[108:111]\n\t"
      "v_exp_f32 v99, v99\n\t"
      "v_mfma_f32_16x16x32_bf16 v[108:111], a[60:63], a[28:31], v[108:111]\n\t"
      "v_add_f32 v28, v28, v98\n\t"
      "v_mfma_f32_16x16x32_bf16 v[192:195], a[64:67], v[112:115], v[192:195]\n\t"
      "v_exp_f32 v100, v100\n\t"
      "v_mfma_f32_16x16x32_bf16 v[224:227], a[64:67], v[120:123], v[224:227]\n\t"
      "v_add_f32 v28, v28, v99\n\t"
      "v_cvt_pk_bf16_f32 v97, v98, v99\n\t"
      "v_mfma_f32_16x16x32_bf16 v[196:199], a[68:71], v[112:115], v[196:199]\n\t"
      "v_exp_f32 v101, v101\n\t"
      "v_mfma_f32_16x16x32_bf16 v[228:231], a[68:71], v[120:123], v[228:231]\n\t"
      "ds_read_b64_tr_b16 a[64:65], v36 offset:16384\n\t"
      "buffer_load_dwordx4 v33, s[24:27], s39 offen offset:1024 nt lds\n\t"
      "s_add_u32 m0, s38, 8192\n\t"
      "v_add_f32 v28, v28, v100\n\t"
      "v_mfma_f32_16x16x32_bf16 v[200:203], a[72:75], v[112:115], v[200:203]\n\t"
      "v_exp_f32 v102, v102\n\t"
      "v_mfma_f32_16x16x32_bf16 v[232:235], a[72:75], v[120:123], v[232:235]\n\t"
      "ds_read_b64_tr_b16 a[66:67], v36 offset:20480\n\t"
      "v_add_f32 v28, v28, v101\n\t"
      "v_cvt_pk_bf16_f32 v98, v100, v101\n\t"
      "v_mfma_f32_16x16x32_bf16 v[204:207], a[76:79], v[112:115], v[204:207]\n\t"
      "v_exp_f32 v103, v103\n\t"
      "v_mfma_f32_16x16x32_bf16 v[236:239], a[76:79], v[120:123], v[236:239]\n\t"
      "ds_read_b64_tr_b16 a[68:69], v37 offset:16384\n\t"
      "v_add_f32 v28, v28, v102\n\t"
      "s_add_u32 s39, s39, 8192\n\t"
      "v_mfma_f32_16x16x32_bf16 v[208:211], a[80:83], v[112:115], v[208:211]\n\t"
      "v_exp_f32 v104, v104\n\t"
      "v_mfma_f32_16x16x32_bf16 v[240:243], a[80:83], v[120:123], v[240:243]\n\t"
      "ds_read_b64_tr_b16 a[70:71], v37 offset:20480\n\t"
      "v_add_f32 v28, v28, v103\n\t"
      "v_cvt_pk_bf16_f32 v99, v102, v103\n\t"
      "v_mfma_f32_16x16x32_bf16 v[212:215], a[84:87], v[112:115], v[212:215]\n\t"
      "v_exp_f32 v105, v105\n\t"
      "v_mfma_f32_16x16x32_bf16 v[244:247], a[84:87], v[120:123], v[244:247]\n\t"
      "ds_read_b64_tr_b16 a[72:73], v38 offset:16384\n\t"
      "v_add_f32 v29, v29, v104\n\t"
      "v_mfma_f32_16x16x32_bf16 v[216:219], a[88:91], v[112:115], v[216:219]\n\t"
      "v_exp_f32 v106, v106\n\t"
      "v_mfma_f32_16x16x32_bf16 v[248:251], a[88:91], v[120:123], v[248:251]\n\t"
      "ds_read_b64_tr_b16 a[74:75], v38 offset:20480\n\t"
      "v_add_f32 v29, v29, v105\n\t"
      "v_cvt_pk_bf16_f32 v104, v104, v105\n\t"
      "v_mfma_f32_16x16x32_bf16 v[220:223], a[92:95], v[112:115], v[220:223]\n\t"
      "v_exp_f32 v107, v107\n\t"
      "v_mfma_f32_16x16x32_bf16 v[252:255], a[92:95], v[120:123], v[252:255]\n\t"
      "ds_read_b64_tr_b16 a[76:77], v39 offset:16384\n\t"
      "buffer_load_dwordx4 v34, s[28:31], s40 offen nt lds\n\t"
      "v_add_f32 v29, v29, v106\n\t"
      "v_mfma_f32_16x16x32_bf16 v[112:115], a[32:35], v[48:51], v[88:91]\n\t"
      "v_exp_f32 v108, v108\n\t"
      "v_mfma_f32_16x16x32_bf16 v[112:115], a[36:39], v[52:55], v[112:115]\n\t"
      "ds_read_b64_tr_b16 a[78:79], v39 offset:20480\n\t"
      "v_add_f32 v29, v29, v107\n\t"
      "v_cvt_pk_bf16_f32 v105, v106, v107\n\t"
      "v_mfma_f32_16x16x32_bf16 v[112:115], a[40:43], v[56:59], v[112:115]\n\t"
      "v_exp_f32 v109, v109\n\t"
      "v_mfma_f32_16x16x32_bf16 v[112:115], a[44:47], v[60:63], v[112:115]\n\t"
      "ds_read_b64_tr_b16 a[80:81], v40 offset:16384\n\t"
      "v_add_f32 v29, v29, v108\n\t"
      "v_mfma_f32_16x16x32_bf16 v[116:119], a[48:51], v[48:51], v[88:91]\n\t"
      "v_exp_f32 v110, v110\n\t"
      "v_mfma_f32_16x16x32_bf16 v[116:119], a[52:55], v[52:55], v[116:119]\n\t"
      "ds_read_b64_tr_b16 a[82:83], v40 offset:20480\n\t"
      "v_add_f32 v29, v29, v109\n\t"
      "v_cvt_pk_bf16_f32 v106, v108, v109\n\t"
      "v_mfma_f32_16x16x32_bf16 v[116:119], a[56:59], v[56:59], v[116:119]\n\t"
      "v_exp_f32 v111, v111\n\t"
      "v_mfma_f32_16x16x32_bf16 v[116:119], a[60:63], v[60:63], v[116:119]\n\t"
      "ds_read_b64_tr_b16 a[84:85], v41 offset:16384\n\t"
      "v_add_f32 v29, v29, v110\n\t"
      "v_mfma_f32_16x16x32_bf16 v[120:123], a[32:35], v[64:67], v[92:95]\n\t"
      "v_exp_f32 v112, v112\n\t"
      "v_mfma_f32_16x16x32_bf16 v[120:123], a[36:39], v[68:71], v[120:123]\n\t"
      "ds_read_b64_tr_b16 a[86:87], v41 offset:20480\n\t"
      "v_add_f32 v29, v29, v111\n\t"
      "v_cvt_pk_bf16_f32 v107, v110, v111\n\t"
      "v_mfma_f32_16x16x32_bf16 v[120:123], a[40:43], v[72:75], v[120:123]\n\t"
      "v_exp_f32 v113, v113\n\t"
      "v_mfma_f32_16x16x32_bf16 v[120:123], a[44:47], v[76:79], v[120:123]\n\t"
      "ds_read_b64_tr_b16 a[88:89], v42 offset:16384\n\t"
      "buffer_load_dwordx4 v35, s[28:31], s40 offen offset:1024 nt lds\n\t"
      "s_mov_b32 m0, s69\n\t"
      "v_add_f32 v30, v30, v112\n\t"
      "v_mfma_f32_16x16x32_bf16 v[124:127], a[48:51], v[64:67], v[92:95]\n\t"
      "v_exp_f32 v114, v114\n\t"
      "v_mfma_f32_16x16x32_bf16 v[124:127], a[52:55], v[68:71], v[124:127]\n\t"
      "ds_read_b64_tr_b16 a[90:91], v42 offset:20480\n\t"
      "v_add_f32 v30, v30, v113\n\t"
      "v_cvt_pk_bf16_f32 v112, v112, v113\n\t"
      "v_mfma_f32_16x16x32_bf16 v[124:127], a[56:59], v[72:75], v[124:127]\n\t"
      "v_exp_f32 v115, v115\n\t"
      "v_mfma_f32_16x16x32_bf16 v[124:127], a[60:63], v[76:79], v[124:127]\n\t"
      "ds_read_b64_tr_b16 a[92:93], v43 offset:16384\n\t"
      "v_add_f32 v30, v30, v114\n\t"
      "s_add_u32 s40, s40, 8192\n\t"
      "s_waitcnt lgkmcnt(7)\n\t"
      "v_mfma_f32_16x16x32_bf16 v[128:131], a[64:67], v[96:99], v[128:131]\n\t"
      "v_exp_f32 v116, v116\n\t"
      "v_mfma_f32_16x16x32_bf16 v[160:163], a[64:67], v[104:107], v[160:163]\n\t"
      "ds_read_b64_tr_b16 a[94:95], v43 offset:20480\n\t"
      "ds_read_b128 a[32:35], v44 offset:24576\n\t"
      "v_add_f32 v30, v30, v115\n\t"
      "v_cvt_pk_bf16_f32 v113, v114, v115\n\t"
      "v_mfma_f32_16x16x32_bf16 v[132:135], a[68:71], v[96:99], v[132:135]\n\t"
      "v_exp_f32 v117, v117\n\t"
      "v_mfma_f32_16x16x32_bf16 v[164:167], a[68:71], v[104:107], v[164:167]\n\t"
      "ds_read_b128 a[36:39], v45 offset:24576\n\t"
      "v_add_f32 v30, v30, v116\n\t"
      "v_mfma_f32_16x16x32_bf16 v[136:139], a[72:75], v[96:99], v[136:139]\n\t"
      "v_exp_f32 v118, v118\n\t"
      "v_mfma_f32_16x16x32_bf16 v[168:171], a[72:75], v[104:107], v[168:171]\n\t"
      "ds_read_b128 a[40:43], v46 offset:24576\n\t"
      "v_add_f32 v30, v30, v117\n\t"
      "v_cvt_pk_bf16_f32 v114, v116, v117\n\t"
      "v_mfma_f32_16x16x32_bf16 v[140:143], a[76:79], v[96:99], v[140:143]\n\t"
      "v_exp_f32 v119, v119\n\t"
      "v_mfma_f32_16x16x32_bf16 v[172:175], a[76:79], v[104:107], v[172:175]\n\t"
      "ds_read_b128 a[44:47], v47 offset:24576\n\t"
      "buffer_load_dwordx4 v34, s[64:67], s70 offen nt lds\n\t"
      "v_add_f32 v30, v30, v118\n\t"
      "s_waitcnt lgkmcnt(4)\n\t"
      "v_mfma_f32_16x16x32_bf16 v[144:147], a[80:83], v[96:99], v[144:147]\n\t"
      "v_exp_f32 v120, v120\n\t"
      "v_mfma_f32_16x16x32_bf16 v[176:179], a[80:83], v[104:107], v[176:179]\n\t"
      "ds_read_b128 a[48:51], v44 offset:28672\n\t"
      "v_add_f32 v30, v30, v119\n\t"
      "v_cvt_pk_bf16_f32 v115, v118, v119\n\t"
      "v_mfma_f32_16x16x32_bf16 v[148:151], a[84:87], v[96:99], v[148:151]\n\t"
      "v_exp_f32 v121, v121\n\t"
      "v_mfma_f32_16x16x32_bf16 v[180:183], a[84:87], v[104:107], v[180:183]\n\t"
      "ds_read_b128 a[52:55], v45 offset:28672\n\t"
      "v_add_f32 v31, v31, v120\n\t"
      "v_mfma_f32_16x16x32_bf16 v[152:155], a[88:91], v[96:99], v[152:155]\n\t"
      "v_exp_f32 v122, v122\n\t"
      "v_mfma_f32_16x16x32_bf16 v[184:187], a[88:91], v[104:107], v[184:187]\n\t"
      "ds_read_b128 a[56:59], v46 offset:28672\n\t"
      "v_add_f32 v31, v31, v121\n\t"
      "v_cvt_pk_bf16_f32 v120, v120, v121\n\t"
      "v_mfma_f32_16x16x32_bf16 v[156:159], a[92:95], v[96:99], v[156:159]\n\t"
      "v_exp_f32 v123, v123\n\t"
      "v_mfma_f32_16x16x32_bf16 v[188:191], a[92:95], v[104:107], v[188:191]\n\t"
      "ds_read_b128 a[60:63], v47 offset:28672\n\t"
      "v_add_f32 v31, v31, v122\n\t"
      "; ---- period u=3\n\t"
      "s_waitcnt lgkmcnt(4)\n\t"
      "v_mfma_f32_16x16x32_bf16 v[96:99], a[32:35], a[0:3], v[80:83]\n\t"
      "v_exp_f32 v124, v124\n\t"
      "v_mfma_f32_16x16x32_bf16 v[96:99], a[36:39], a[4:7], v[96:99]\n\t"
      "v_add_f32 v31, v31, v123\n\t"
      "v_cvt_pk_bf16_f32 v121, v122, v123\n\t"
      "v_mfma_f32_16x16x32_bf16 v[96:99], a[40:43], a[8:11], v[96:99]\n\t"
      "v_exp_f32 v125, v125\n\t"
      "v_mfma_f32_16x16x32_bf16 v[96:99], a[44:47], a[12:15], v[96:99]\n\t"
      "v_add_f32 v31, v31, v124\n\t"
      "s_waitcnt lgkmcnt(0)\n\t"
      "v_mfma_f32_16x16x32_bf16 v[100:103], a[48:51], a[0:3], v[80:83]\n\t"
      "s_add_u32 m0, s37, 24576\n\t"
      "v_exp_f32 v126, v126\n\t"
      "v_mfma_f32_16x16x32_bf16 v[100:103], a[52:55], a[4:7], v[100:103]\n\t"
      "v_add_f32 v31, v31, v125\n\t"
      "v_cvt_pk_bf16_f32 v122, v124, v125\n\t"
      "v_mfma_f32_16x16x32_bf16 v[100:103], a[56:59], a[8:11], v[100:103]\n\t"
      "s_waitcnt vmcnt(11)\n\t"
      "s_barrier\n\t"
      "v_exp_f32 v127, v127\n\t"
      "v_mfma_f32_16x16x32_bf16 v[100:103], a[60:63], a[12:15], v[100:103]\n\t"
      "buffer_load_dwordx4 v32, s[24:27], s39 offen nt lds\n\t"
      "v_add_f32 v31, v31, v126\n\t"
      "v_mfma_f32_16x16x32_bf16 v[104:107], a[32:35], a[16:19], v[84:87]\n\t"
      "v_exp_f32 v96, v96\n\t"
      "v_mfma_f32_16x16x32_bf16 v[104:107], a[36:39], a[20:23], v[104:107]\n\t"
      "v_add_f32 v31, v31, v127\n\t"
      "v_cvt_pk_bf16_f32 v123, v126, v127\n\t"
      "v_mfma_f32_16x16x32_bf16 v[104:107], a[40:43], a[24:27], v[104:107]\n\t"
      "v_exp_f32 v97, v97\n\t"
      "v_mfma_f32_16x16x32_bf16 v[104:107], a[44:47], a[28:31], v[104:107]\n\t"
      "v_add_f32 v28, v28, v96\n\t"
      "v_mfma_f32_16x16x32_bf16 v[108:111], a[48:51], a[16:19], v[84:87]\n\t"
      "v_exp_f32 v98, v98\n\t"
      "v_mfma_f32_16x16x32_bf16 v[108:111], a[52:55], a[20:23], v[108:111]\n\t"
      "v_add_f32 v28, v28, v97\n\t"
      "v_cvt_pk_bf16_f32 v96, v96, v97\n\t"
      "v_mfma_f32_16x16x32_bf16 v[108:111], a[56:59], a[24:27], v[108:111]\n\t"
      "v_exp_f32 v99, v99\n\t"
      "v_mfma_f32_16x16x32_bf16 v[108:111], a[60:63], a[28:31], v[108:111]\n\t"
      "v_add_f32 v28, v28, v98\n\t"
      "v_mfma_f32_16x16x32_bf16 v[192:195], a[64:67], v[112:115], v[192:195]\n\t"
      "v_exp_f32 v100, v100\n\t"
      "v_mfma_f32_16x16x32_bf16 v[224:227], a[64:67], v[120:123], v[224:227]\n\t"
      "v_add_f32 v28, v28, v99\n\t"
      "v_cvt_pk_bf16_f32 v97, v98, v99\n\t"
      "v_mfma_f32_16x16x32_bf16 v[196:199], a[68:71], v[112:115], v[196:199]\n\t"
      "v_exp_f32 v101, v101\n\t"
      "v_mfma_f32_16x16x32_bf16 v[228:231], a[68:71], v[120:123], v[228:231]\n\t"
      "ds_read_b64_tr_b16 a[64:65], v36 offset:24576\n\t"
      "buffer_load_dwordx4 v33, s[24:27], s39 offen offset:1024 nt lds\n\t"
      "s_cmp_eq_u32 s41, 2\n\t"
      "s_cselect_b32 s30, 0, s30\n\t"
      "s_add_u32 m0, s38, 16384\n\t"
      "v_add_f32 v28, v28, v100\n\t"
      "v_mfma_f32_16x16x32_bf16 v[200:203], a[72:75], v[112:115], v[200:203]\n\t"
      "v_exp_f32 v102, v102\n\t"
      "v_mfma_f32_16x16x32_bf16 v[232:235], a[72:75], v[120:123], v[232:235]\n\t"
      "ds_read_b64_tr_b16 a[66:67], v36 offset:28672\n\t"
      "v_add_f32 v28, v28, v101\n\t"
      "v_cvt_pk_bf16_f32 v98, v100, v101\n\t"
      "v_mfma_f32_16x16x32_bf16 v[204:207], a[76:79], v[112:115], v[204:207]\n\t"
      "v_exp_f32 v103, v103\n\t"
      "v_mfma_f32_16x16x32_bf16 v[236:239], a[76:79], v[120:123], v[236:239]\n\t"
      "ds_read_b64_tr_b16 a[68:69], v37 offset:24576\n\t"
      "v_add_f32 v28, v28, v102\n\t"
      "s_add_u32 s39, s39, 8192\n\t"
      "v_mfma_f32_16x16x32_bf16 v[208:211], a[80:83], v[112:115], v[208:211]\n\t"
      "v_exp_f32 v104, v104\n\t"
      "v_mfma_f32_16x16x32_bf16 v[240:243], a[80:83], v[120:123], v[240:243]\n\t"
      "ds_read_b64_tr_b16 a[70:71], v37 offset:28672\n\t"
      "v_add_f32 v28, v28, v103\n\t"
      "v_cvt_pk_bf16_f32 v99, v102, v103\n\t"
      "v_mfma_f32_16x16x32_bf16 v[212:215], a[84:87], v[112:115], v[212:215]\n\t"
      "v_exp_f32 v105, v105\n\t"
      "v_mfma_f32_16x16x32_bf16 v[244:247], a[84:87], v[120:123], v[244:247]\n\t"
      "ds_read_b64_tr_b16 a[72:73], v38 offset:24576\n\t"
      "v_add_f32 v29, v29, v104\n\t"
      "v_mfma_f32_16x16x32_bf16 v[216:219], a[88:91], v[112:115], v[216:219]\n\t"
      "v_exp_f32 v106, v106\n\t"
      "v_mfma_f32_16x16x32_bf16 v[248:251], a[88:91], v[120:123], v[248:251]\n\t"
      "ds_read_b64_tr_b16 a[74:75], v38 offset:28672\n\t"
      "v_add_f32 v29, v29, v105\n\t"
      "v_cvt_pk_bf16_f32 v104, v104, v105\n\t"
      "v_mfma_f32_16x16x32_bf16 v[220:223], a[92:95], v[112:115], v[220:223]\n\t"
      "v_exp_f32 v107, v107\n\t"
      "v_mfma_f32_16x16x32_bf16 v[252:255], a[92:95], v[120:123], v[252:255]\n\t"
      "ds_read_b64_tr_b16 a[76:77], v39 offset:24576\n\t"
      "buffer_load_dwordx4 v34, s[28:31], s40 offen nt lds\n\t"
      "v_add_f32 v29, v29, v106\n\t"
      "v_mfma_f32_16x16x32_bf16 v[112:115], a[32:35], v[48:51], v[88:91]\n\t"
      "v_exp_f32 v108, v108\n\t"
      "v_mfma_f32_16x16x32_bf16 v[112:115], a[36:39], v[52:55], v[112:115]\n\t"
      "ds_read_b64_tr_b16 a[78:79], v39 offset:28672\n\t"
      "v_add_f32 v29, v29, v107\n\t"
      "v_cvt_pk_bf16_f32 v105, v106, v107\n\t"
      "v_mfma_f32_16x16x32_bf16 v[112:115], a[40:43], v[56:59], v[112:115]\n\t"
      "v_exp_f32 v109, v109\n\t"
      "v_mfma_f32_16x16x32_bf16 v[112:115], a[44:47], v[60:63], v[112:115]\n\t"
      "ds_read_b64_tr_b16 a[80:81], v40 offset:24576\n\t"
      "v_add_f32 v29, v29, v108\n\t"
      "v_mfma_f32_16x16x32_bf16 v[116:119], a[48:51], v[48:51], v[88:91]\n\t"
      "v_exp_f32 v110, v110\n\t"
      "v_mfma_f32_16x16x32_bf16 v[116:119], a[52:55], v[52:55], v[116:119]\n\t"
      "ds_read_b64_tr_b16 a[82:83], v40 offset:28672\n\t"
      "v_add_f32 v29, v29, v109\n\t"
      "v_cvt_pk_bf16_f32 v106, v108, v109\n\t"
      "v_mfma_f32_16x16x32_bf16 v[116:119], a[56:59], v[56:59], v[116:119]\n\t"
      "v_exp_f32 v111, v111\n\t"
      "v_mfma_f32_16x16x32_bf16 v[116:119], a[60:63], v[60:63], v[116:119]\n\t"
      "ds_read_b64_tr_b16 a[84:85], v41 offset:24576\n\t"
      "v_add_f32 v29, v29, v110\n\t"
      "v_mfma_f32_16x16x32_bf16 v[120:123], a[32:35], v[64:67], v[92:95]\n\t"
      "v_exp_f32 v112, v112\n\t"
      "v_mfma_f32_16x16x32_bf16 v[120:123], a[36:39], v[68:71], v[120:123]\n\t"
      "ds_read_b64_tr_b16 a[86:87], v41 offset:28672\n\t"
      "v_add_f32 v29, v29, v111\n\t"
      "v_cvt_pk_bf16_f32 v107, v110, v111\n\t"
      "v_mfma_f32_16x16x32_bf16 v[120:123], a[40:43], v[72:75], v[120:123]\n\t"
      "v_exp_f32 v113, v113\n\t"
      "v_mfma_f32_16x16x32_bf16 v[120:123], a[44:47], v[76:79], v[120:123]\n\t"
      "ds_read_b64_tr_b16 a[88:89], v42 offset:24576\n\t"
      "buffer_load_dwordx4 v35, s[28:31], s40 offen offset:1024 nt lds\n\t"
      "s_mov_b32 m0, s69\n\t"
      "v_add_f32 v30, v30, v112\n\t"
      "v_mfma_f32_16x16x32_bf16 v[124:127], a[48:51], v[64:67], v[92:95]\n\t"
      "v_exp_f32 v114, v114\n\t"
      "v_mfma_f32_16x16x32_bf16 v[124:127], a[52:55], v[68:71], v[124:127]\n\t"
      "ds_read_b64_tr_b16 a[90:91], v42 offset:28672\n\t"
      "v_add_f32 v30, v30, v113\n\t"
      "v_cvt_pk_bf16_f32 v112, v112, v113\n\t"
      "v_mfma_f32_16x16x32_bf16 v[124:127], a[56:59], v[72:75], v[124:127]\n\t"
      "v_exp_f32 v115, v115\n\t"
      "v_mfma_f32_16x16x32_bf16 v[124:127], a[60:63], v[76:79], v[124:127]\n\t"
      "ds_read_b64_tr_b16 a[92:93], v43 offset:24576\n\t"
      "v_add_f32 v30, v30, v114\n\t"
      "s_add_u32 s40, s40, 8192\n\t"
      "s_waitcnt lgkmcnt(7)\n\t"
      "v_mfma_f32_16x16x32_bf16 v[128:131], a[64:67], v[96:99], v[128:131]\n\t"
      "v_exp_f32 v116, v116\n\t"
      "v_mfma_f32_16x16x32_bf16 v[160:163], a[64:67], v[104:107], v[160:163]\n\t"
      "ds_read_b64_tr_b16 a[94:95], v43 offset:28672\n\t"
      "ds_read_b128 a[32:35], v44 offset:0\n\t"
      "v_add_f32 v30, v30, v115\n\t"
      "v_cvt_pk_bf16_f32 v113, v114, v115\n\t"
      "v_mfma_f32_16x16x32_bf16 v[132:135], a[68:71], v[96:99], v[132:135]\n\t"
      "v_exp_f32 v117, v117\n\t"
      "v_mfma_f32_16x16x32_bf16 v[164:167], a[68:71], v[104:107], v[164:167]\n\t"
      "ds_read_b128 a[36:39], v45 offset:0\n\t"
      "v_add_f32 v30, v30, v116\n\t"
      "v_mfma_f32_16x16x32_bf16 v[136:139], a[72:75], v[96:99], v[136:139]\n\t"
      "v_exp_f32 v118, v118\n\t"
      "v_mfma_f32_16x16x32_bf16 v[168:171], a[72:75], v[104:107], v[168:171]\n\t"
      "ds_read_b128 a[40:43], v46 offset:0\n\t"
      "v_add_f32 v30, v30, v117\n\t"
      "v_cvt_pk_bf16_f32 v114, v116, v117\n\t"
      "v_mfma_f32_16x16x32_bf16 v[140:143], a[76:79], v[96:99], v[140:143]\n\t"
      "v_exp_f32 v119, v119\n\t"
      "v_mfma_f32_16x16x32_bf16 v[172:175], a[76:79], v[104:107], v[172:175]\n\t"
      "ds_read_b128 a[44:47], v47 offset:0\n\t"
      "buffer_load_dwordx4 v35, s[64:67], s70 offen offset:1024 nt lds\n\t"
      "v_add_f32 v30, v30, v118\n\t"
      "s_waitcnt lgkmcnt(4)\n\t"
      "v_mfma_f32_16x16x32_bf16 v[144:147], a[80:83], v[96:99], v[144:147]\n\t"
      "v_exp_f32 v120, v120\n\t"
      "v_mfma_f32_16x16x32_bf16 v[176:179], a[80:83], v[104:107], v[176:179]\n\t"
      "ds_read_b128 a[48:51], v44 offset:4096\n\t"
      "v_add_f32 v30, v30, v119\n\t"
      "v_cvt_pk_bf16_f32 v115, v118, v119\n\t"
      "v_mfma_f32_16x16x32_bf16 v[148:151], a[84:87], v[96:99], v[148:151]\n\t"
      "v_exp_f32 v121, v121\n\t"
      "v_mfma_f32_16x16x32_bf16 v[180:183], a[84:87], v[104:107], v[180:183]\n\t"
      "ds_read_b128 a[52:55], v45 offset:4096\n\t"
      "v_add_f32 v31, v31, v120\n\t"
      "v_mfma_f32_16x16x32_bf16 v[152:155], a[88:91], v[96:99], v[152:155]\n\t"
      "v_exp_f32 v122, v122\n\t"
      "v_mfma_f32_16x16x32_bf16 v[184:187], a[88:91], v[104:107], v[184:187]\n\t"
      "ds_read_b128 a[56:59], v46 offset:4096\n\t"
      "v_add_f32 v31, v31, v121\n\t"
      "v_cvt_pk_bf16_f32 v120, v120, v121\n\t"
      "v_mfma_f32_16x16x32_bf16 v[156:159], a[92:95], v[96:99], v[156:159]\n\t"
      "v_exp_f32 v123, v123\n\t"
      "v_mfma_f32_16x16x32_bf16 v[188:191], a[92:95], v[104:107], v[188:191]\n\t"
      "ds_read_b128 a[60:63], v47 offset:4096\n\t"
      "v_add_f32 v31, v31, v122\n\t"
      "s_cmp_eq_u32 s41, 2\n\t"
      "s_cbranch_scc1 TAIL%=\n\t"
      "s_cmp_lt_u32 s41, 2\n\t"
      "s_cselect_b32 s62, 131072, 0\n\t"
      "s_cselect_b32 s66, 131072, 0\n\t"
      "s_lshl_b32 s42, s41, 13\n\t"
      "s_add_u32 s43, s37, s42\n\t"
      "s_add_u32 s43, s43, 32768\n\t"
      "s_cmp_lt_u32 s41, 2\n\t"
      "s_cselect_b32 s68, s43, 196608\n\t"
      "s_add_u32 s43, s43, 49152\n\t"
      "s_cmp_lt_u32 s41, 2\n\t"
      "s_cselect_b32 s69, s43, 196608\n\t"
      "s_add_u32 s70, s42, 114688\n\t"
      "s_add_u32 s70, s70, s71\n\t"
      "s_and_b32 s70, s70, 0x1ffff\n\t"
      "s_add_u32 s41, s41, 1\n\t"
      "s_branch LOOP%=\n\t"
      "TAIL%=:\n\t"
      "s_waitcnt lgkmcnt(2)\n\t"
      "v_mfma_f32_16x16x32_bf16 v[96:99], a[32:35], a[0:3], v[80:83]\n\t"
      "v_exp_f32 v124, v124\n\t"
      "v_mfma_f32_16x16x32_bf16 v[96:99], a[36:39], a[4:7], v[96:99]\n\t"
      "v_add_f32 v31, v31, v123\n\t"
      "v_cvt_pk_bf16_f32 v121, v122, v123\n\t"
      "v_mfma_f32_16x16x32_bf16 v[96:99], a[40:43], a[8:11], v[96:99]\n\t"
      "v_exp_f32 v125, v125\n\t"
      "v_mfma_f32_16x16x32_bf16 v[96:99], a[44:47], a[12:15], v[96:99]\n\t"
      "v_add_f32 v31, v31, v124\n\t"
      "v_mfma_f32_16x16x32_bf16 v[100:103], a[48:51], a[0:3], v[80:83]\n\t"
      "v_exp_f32 v126, v126\n\t"
      "v_mfma_f32_16x16x32_bf16 v[100:103], a[52:55], a[4:7], v[100:103]\n\t"
      "v_add_f32 v31, v31, v125\n\t"
      "v_cvt_pk_bf16_f32 v122, v124, v125\n\t"
      "s_waitcnt lgkmcnt(0)\n\t"
      "v_mfma_f32_16x16x32_bf16 v[100:103], a[56:59], a[8:11], v[100:103]\n\t"
      "s_waitcnt vmcnt(6)\n\t"
      "s_barrier\n\t"
      "v_exp_f32 v127, v127\n\t"
      "v_mfma_f32_16x16x32_bf16 v[100:103], a[60:63], a[12:15], v[100:103]\n\t"
      "v_add_f32 v31, v31, v126\n\t"
      "v_mfma_f32_16x16x32_bf16 v[104:107], a[32:35], a[16:19], v[84:87]\n\t"
      "v_exp_f32 v96, v96\n\t"
      "v_mfma_f32_16x16x32_bf16 v[104:107], a[36:39], a[20:23], v[104:107]\n\t"
      "v_add_f32 v31, v31, v127\n\t"
      "v_cvt_pk_bf16_f32 v123, v126, v127\n\t"
      "v_mfma_f32_16x16x32_bf16 v[104:107], a[40:43], a[24:27], v[104:107]\n\t"
      "v_exp_f32 v97, v97\n\t"
      "v_mfma_f32_16x16x32_bf16 v[104:107], a[44:47], a[28:31], v[104:107]\n\t"
      "v_add_f32 v28, v28, v96\n\t"
      "v_mfma_f32_16x16x32_bf16 v[108:111], a[48:51], a[16:19], v[84:87]\n\t"
      "ds_read_b128 a[96:99], v44 offset:8192\n\t"
      "v_exp_f32 v98, v98\n\t"
      "v_mfma_f32_16x16x32_bf16 v[108:111], a[52:55], a[20:23], v[108:111]\n\t"
      "ds_read_b64_tr_b16 a[128:129], v36 offset:0\n\t"
      "v_add_f32 v28, v28, v97\n\t"
      "v_cvt_pk_bf16_f32 v96, v96, v97\n\t"
      "v_mfma_f32_16x16x32_bf16 v[108:111], a[56:59], a[24:27], v[108:111]\n\t"
      "ds_read_b128 a[100:103], v45 offset:8192\n\t"
      "v_exp_f32 v99, v99\n\t"
      "v_mfma_f32_16x16x32_bf16 v[108:111], a[60:63], a[28:31], v[108:111]\n\t"
      "ds_read_b64_tr_b16 a[130:131], v36 offset:4096\n\t"
      "v_add_f32 v28, v28, v98\n\t"
      "v_mfma_f32_16x16x32_bf16 v[192:195], a[64:67], v[112:115], v[192:195]\n\t"
      "ds_read_b128 a[104:107], v46 offset:8192\n\t"
      "v_exp_f32 v100, v100\n\t"
      "v_mfma_f32_16x16x32_bf16 v[224:227], a[64:67], v[120:123], v[224:227]\n\t"
      "ds_read_b64_tr_b16 a[132:133], v37 offset:0\n\t"
      "v_add_f32 v28, v28, v99\n\t"
      "v_cvt_pk_bf16_f32 v97, v98, v99\n\t"
      "v_mfma_f32_16x16x32_bf16 v[196:199], a[68:71], v[112:115], v[196:199]\n\t"
      "ds_read_b128 a[108:111], v47 offset:8192\n\t"
      "v_exp_f32 v101, v101\n\t"
      "v_mfma_f32_16x16x32_bf16 v[228:231], a[68:71], v[120:123], v[228:231]\n\t"
      "ds_read_b64_tr_b16 a[134:135], v37 offset:4096\n\t"
      "v_add_f32 v28, v28, v100\n\t"
      "v_mfma_f32_16x16x32_bf16 v[200:203], a[72:75], v[112:115], v[200:203]\n\t"
      "ds_read_b128 a[112:115], v44 offset:12288\n\t"
      "v_exp_f32 v102, v102\n\t"
      "v_mfma_f32_16x16x32_bf16 v[232:235], a[72:75], v[120:123], v[232:235]\n\t"
      "ds_read_b64_tr_b16 a[136:137], v38 offset:0\n\t"
      "v_add_f32 v28, v28, v101\n\t"
      "v_cvt_pk_bf16_f32 v98, v100, v101\n\t"
      "v_mfma_f32_16x16x32_bf16 v[204:207], a[76:79], v[112:115], v[204:207]\n\t"
      "ds_read_b128 a[116:119], v45 offset:12288\n\t"
      "v_exp_f32 v103, v103\n\t"
      "v_mfma_f32_16x16x32_bf16 v[236:239], a[76:79], v[120:123], v[236:239]\n\t"
      "ds_read_b64_tr_b16 a[138:139], v38 offset:4096\n\t"
      "v_add_f32 v28, v28, v102\n\t"
      "v_mfma_f32_16x16x32_bf16 v[208:211], a[80:83], v[112:115], v[208:211]\n\t"
      "ds_read_b128 a[120:123], v46 offset:12288\n\t"
      "v_exp_f32 v104, v104\n\t"
      "v_mfma_f32_16x16x32_bf16 v[240:243], a[80:83], v[120:123], v[240:243]\n\t"
      "ds_read_b64_tr_b16 a[140:141], v39 offset:0\n\t"
      "v_add_f32 v28, v28, v103\n\t"
      "v_cvt_pk_bf16_f32 v99, v102, v103\n\t"
      "v_mfma_f32_16x16x32_bf16 v[212:215], a[84:87], v[112:115], v[212:215]\n\t"
      "ds_read_b128 a[124:127], v47 offset:12288\n\t"
      "v_exp_f32 v105, v105\n\t"
      "v_mfma_f32_16x16x32_bf16 v[244:247], a[84:87], v[120:123], v[244:247]\n\t"
      "ds_read_b64_tr_b16 a[142:143], v39 offset:4096\n\t"
      "v_add_f32 v29, v29, v104\n\t"
      "v_mfma_f32_16x16x32_bf16 v[216:219], a[88:91], v[112:115], v[216:219]\n\t"
      "v_exp_f32 v106, v106\n\t"
      "v_mfma_f32_16x16x32_bf16 v[248:251], a[88:91], v[120:123], v[248:251]\n\t"
      "ds_read_b64_tr_b16 a[144:145], v40 offset:0\n\t"
      "v_add_f32 v29, v29, v105\n\t"
      "v_cvt_pk_bf16_f32 v104, v104, v105\n\t"
      "v_mfma_f32_16x16x32_bf16 v[220:223], a[92:95], v[112:115], v[220:223]\n\t"
      "v_exp_f32 v107, v107\n\t"
      "v_mfma_f32_16x16x32_bf16 v[252:255], a[92:95], v[120:123], v[252:255]\n\t"
      "ds_read_b64_tr_b16 a[146:147], v40 offset:4096\n\t"
      "v_add_f32 v29, v29, v106\n\t"
      "s_waitcnt lgkmcnt(2)\n\t"
      "v_mfma_f32_16x16x32_bf16 v[112:115], a[96:99], a[0:3], v[80:83]\n\t"
      "v_exp_f32 v108, v108\n\t"
      "v_mfma_f32_16x16x32_bf16 v[112:115], a[100:103], a[4:7], v[112:115]\n\t"
      "ds_read_b64_tr_b16 a[148:149], v41 offset:0\n\t"
      "v_add_f32 v29, v29, v107\n\t"
      "v_cvt_pk_bf16_f32 v105, v106, v107\n\t"
      "v_mfma_f32_16x16x32_bf16 v[112:115], a[104:107], a[8:11], v[112:115]\n\t"
      "v_exp_f32 v109, v109\n\t"
      "v_mfma_f32_16x16x32_bf16 v[112:115], a[108:111], a[12:15], v[112:115]\n\t"
      "ds_read_b64_tr_b16 a[150:151], v41 offset:4096\n\t"
      "v_add_f32 v29, v29, v108\n\t"
      "v_mfma_f32_16x16x32_bf16 v[116:119], a[112:115], a[0:3], v[80:83]\n\t"
      "v_exp_f32 v110, v110\n\t"
      "v_mfma_f32_16x16x32_bf16 v[116:119], a[116:119], a[4:7], v[116:119]\n\t"
      "ds_read_b64_tr_b16 a[152:153], v42 offset:0\n\t"
      "v_add_f32 v29, v29, v109\n\t"
      "v_cvt_pk_bf16_f32 v106, v108, v109\n\t"
      "v_mfma_f32_16x16x32_bf16 v[116:119], a[120:123], a[8:11], v[116:119]\n\t"
      "v_exp_f32 v111, v111\n\t"
      "v_mfma_f32_16x16x32_bf16 v[116:119], a[124:127], a[12:15], v[116:119]\n\t"
      "ds_read_b64_tr_b16 a[154:155], v42 offset:4096\n\t"
      "v_add_f32 v29, v29, v110\n\t"
      "v_mfma_f32_16x16x32_bf16 v[120:123], a[96:99], a[16:19], v[84:87]\n\t"
      "v_exp_f32 v112, v112\n\t"
      "v_mfma_f32_16x16x32_bf16 v[120:123], a[100:103], a[20:23], v[120:123]\n\t"
      "ds_read_b64_tr_b16 a[156:157], v43 offset:0\n\t"
      "v_add_f32 v29, v29, v111\n\t"
      "v_cvt_pk_bf16_f32 v107, v110, v111\n\t"
      "v_mfma_f32_16x16x32_bf16 v[120:123], a[104:107], a[24:27], v[120:123]\n\t"
      "v_exp_f32 v113, v113\n\t"
      "v_mfma_f32_16x16x32_bf16 v[120:123], a[108:111], a[28:31], v[120:123]\n\t"
      "ds_read_b64_tr_b16 a[158:159], v43 offset:4096\n\t"
      "v_add_f32 v28, v28, v112\n\t"
      "v_mfma_f32_16x16x32_bf16 v[124:127], a[112:115], a[16:19], v[84:87]\n\t"
      "ds_read_b128 a[32:35], v44 offset:32768\n\t"
      "v_exp_f32 v114, v114\n\t"
      "v_mfma_f32_16x16x32_bf16 v[124:127], a[116:119], a[20:23], v[124:127]\n\t"
      "ds_read_b64_tr_b16 a[64:65], v36 offset:8192\n\t"
      "v_add_f32 v28, v28, v113\n\t"
      "v_cvt_pk_bf16_f32 v112, v112, v113\n\t"
      "v_mfma_f32_16x16x32_bf16 v[124:127], a[120:123], a[24:27], v[124:127]\n\t"
      "ds_read_b128 a[36:39], v45 offset:32768\n\t"
      "v_exp_f32 v115, v115\n\t"
      "v_mfma_f32_16x16x32_bf16 v[124:127], a[124:127], a[28:31], v[124:127]\n\t"
      "ds_read_b64_tr_b16 a[66:67], v36 offset:12288\n\t"
      "v_add_f32 v28, v28, v114\n\t"
      "v_mfma_f32_16x16x32_bf16 v[128:131], a[128:131], v[96:99], v[128:131]\n\t"
      "ds_read_b128 a[40:43], v46 offset:32768\n\t"
      "v_exp_f32 v116, v116\n\t"
      "v_mfma_f32_16x16x32_bf16 v[160:163], a[128:131], v[104:107], v[160:163]\n\t"
      "ds_read_b64_tr_b16 a[68:69], v37 offset:8192\n\t"
      "v_add_f32 v28, v28, v115\n\t"
      "v_cvt_pk_bf16_f32 v113, v114, v115\n\t"
      "v_mfma_f32_16x16x32_bf16 v[132:135], a[132:135], v[96:99], v[132:135]\n\t"
      "ds_read_b128 a[44:47], v47 offset:32768\n\t"
      "v_exp_f32 v117, v117\n\t"
      "v_mfma_f32_16x16x32_bf16 v[164:167], a[132:135], v[104:107], v[164:167]\n\t"
      "ds_read_b64_tr_b16 a[70:71], v37 offset:12288\n\t"
      "v_add_f32 v28, v28, v116\n\t"
      "v_mfma_f32_16x16x32_bf16 v[136:139], a[136:139], v[96:99], v[136:139]\n\t"
      "ds_read_b128 a[48:51], v44 offset:36864\n\t"
      "v_exp_f32 v118, v118\n\t"
      "v_mfma_f32_16x16x32_bf16 v[168:171], a[136:139], v[104:107], v[168:171]\n\t"
      "ds_read_b64_tr_b16 a[72:73], v38 offset:8192\n\t"
      "v_add_f32 v28, v28, v117\n\t"
      "v_cvt_pk_bf16_f32 v114, v116, v117\n\t"
      "v_mfma_f32_16x16x32_bf16 v[140:143], a[140:143], v[96:99], v[140:143]\n\t"
      "ds_read_b128 a[52:55], v45 offset:36864\n\t"
      "v_exp_f32 v119, v119\n\t"
      "v_mfma_f32_16x16x32_bf16 v[172:175], a[140:143], v[104:107], v[172:175]\n\t"
      "ds_read_b64_tr_b16 a[74:75], v38 offset:12288\n\t"
      "v_add_f32 v28, v28, v118\n\t"
      "s_waitcnt lgkmcnt(4)\n\t"
      "v_mfma_f32_16x16x32_bf16 v[144:147], a[144:147], v[96:99], v[144:147]\n\t"
      "ds_read_b128 a[56:59], v46 offset:36864\n\t"
      "v_exp_f32 v120, v120\n\t"
      "v_mfma_f32_16x16x32_bf16 v[176:179], a[144:147], v[104:107], v[176:179]\n\t"
      "ds_read_b64_tr_b16 a[76:77], v39 offset:8192\n\t"
      "v_add_f32 v28, v28, v119\n\t"
      "v_cvt_pk_bf16_f32 v115, v118, v119\n\t"
      "v_mfma_f32_16x16x32_bf16 v[148:151], a[148:151], v[96:99], v[148:151]\n\t"
      "ds_read_b128 a[60:63], v47 offset:36864\n\t"
      "v_exp_f32 v121, v121\n\t"
      "v_mfma_f32_16x16x32_bf16 v[180:183], a[148:151], v[104:107], v[180:183]\n\t"
      "ds_read_b64_tr_b16 a[78:79], v39 offset:12288\n\t"
      "v_add_f32 v29, v29, v120\n\t"
      "v_mfma_f32_16x16x32_bf16 v[152:155], a[152:155], v[96:99], v[152:155]\n\t"
      "v_exp_f32 v122, v122\n\t"
      "v_mfma_f32_16x16x32_bf16 v[184:187], a[152:155], v[104:107], v[184:187]\n\t"
      "ds_read_b64_tr_b16 a[80:81], v40 offset:8192\n\t"
      "v_add_f32 v29, v29, v121\n\t"
      "v_cvt_pk_bf16_f32 v120, v120, v121\n\t"
      "v_mfma_f32_16x16x32_bf16 v[156:159], a[156:159], v[96:99], v[156:159]\n\t"
      "v_exp_f32 v123, v123\n\t"
      "v_mfma_f32_16x16x32_bf16 v[188:191], a[156:159], v[104:107], v[188:191]\n\t"
      "ds_read_b64_tr_b16 a[82:83], v40 offset:12288\n\t"
      "v_add_f32 v29, v29, v122\n\t"
      "v_mfma_f32_16x16x32_bf16 v[96:99], a[32:35], a[0:3], v[80:83]\n\t"
      "v_exp_f32 v124, v124\n\t"
      "v_mfma_f32_16x16x32_bf16 v[96:99], a[36:39], a[4:7], v[96:99]\n\t"
      "ds_read_b64_tr_b16 a[84:85], v41 offset:8192\n\t"
      "v_add_f32 v29, v29, v123\n\t"
      "v_cvt_pk_bf16_f32 v121, v122, v123\n\t"
      "v_mfma_f32_16x16x32_bf16 v[96:99], a[40:43], a[8:11], v[96:99]\n\t"
      "v_exp_f32 v125, v125\n\t"
      "v_mfma_f32_16x16x32_bf16 v[96:99], a[44:47], a[12:15], v[96:99]\n\t"
      "ds_read_b64_tr_b16 a[86:87], v41 offset:12288\n\t"
      "v_add_f32 v29, v29, v124\n\t"
      "s_waitcnt lgkmcnt(2)\n\t"
      "v_mfma_f32_16x16x32_bf16 v[100:103], a[48:51], a[0:3], v[80:83]\n\t"
      "v_exp_f32 v126, v126\n\t"
      "v_mfma_f32_16x16x32_bf16 v[100:103], a[52:55], a[4:7], v[100:103]\n\t"
      "ds_read_b64_tr_b16 a[88:89], v42 offset:8192\n\t"
      "v_add_f32 v29, v29, v125\n\t"
      "v_cvt_pk_bf16_f32 v122, v124, v125\n\t"
      "v_mfma_f32_16x16x32_bf16 v[100:103], a[56:59], a[8:11], v[100:103]\n\t"
      "v_exp_f32 v127, v127\n\t"
      "v_mfma_f32_16x16x32_bf16 v[100:103], a[60:63], a[12:15], v[100:103]\n\t"
      "ds_read_b64_tr_b16 a[90:91], v42 offset:12288\n\t"
      "v_add_f32 v29, v29, v126\n\t"
      "v_mfma_f32_16x16x32_bf16 v[104:107], a[32:35], a[16:19], v[84:87]\n\t"
      "v_exp_f32 v96, v96\n\t"
      "v_mfma_f32_16x16x32_bf16 v[104:107], a[36:39], a[20:23], v[104:107]\n\t"
      "ds_read_b64_tr_b16 a[92:93], v43 offset:8192\n\t"
      "v_add_f32 v29, v29, v127\n\t"
      "v_cvt_pk_bf16_f32 v123, v126, v127\n\t"
      "v_mfma_f32_16x16x32_bf16 v[104:107], a[40:43], a[24:27], v[104:107]\n\t"
      "v_exp_f32 v97, v97\n\t"
      "v_mfma_f32_16x16x32_bf16 v[104:107], a[44:47], a[28:31], v[104:107]\n\t"
      "ds_read_b64_tr_b16 a[94:95], v43 offset:12288\n\t"
      "v_add_f32 v28, v28, v96\n\t"
      "v_mfma_f32_16x16x32_bf16 v[108:111], a[48:51], a[16:19], v[84:87]\n\t"
      "ds_read_b128 a[96:99], v44 offset:40960\n\t"
      "v_exp_f32 v98, v98\n\t"
      "v_mfma_f32_16x16x32_bf16 v[108:111], a[52:55], a[20:23], v[108:111]\n\t"
      "ds_read_b64_tr_b16 a[128:129], v36 offset:32768\n\t"
      "v_add_f32 v28, v28, v97\n\t"
      "v_cvt_pk_bf16_f32 v96, v96, v97\n\t"
      "v_mfma_f32_16x16x32_bf16 v[108:111], a[56:59], a[24:27], v[108:111]\n\t"
      "ds_read_b128 a[100:103], v45 offset:40960\n\t"
      "v_exp_f32 v99, v99\n\t"
      "v_mfma_f32_16x16x32_bf16 v[108:111], a[60:63], a[28:31], v[108:111]\n\t"
      "ds_read_b64_tr_b16 a[130:131], v36 offset:36864\n\t"
      "v_add_f32 v28, v28, v98\n\t"
      "v_mfma_f32_16x16x32_bf16 v[128:131], a[64:67], v[112:115], v[128:131]\n\t"
      "ds_read_b128 a[104:107], v46 offset:40960\n\t"
      "v_exp_f32 v100, v100\n\t"
      "v_mfma_f32_16x16x32_bf16 v[160:163], a[64:67], v[120:123], v[160:163]\n\t"
      "ds_read_b64_tr_b16 a[132:133], v37 offset:32768\n\t"
      "v_add_f32 v28, v28, v99\n\t"
      "v_cvt_pk_bf16_f32 v97, v98, v99\n\t"
      "v_mfma_f32_16x16x32_bf16 v[132:135], a[68:71], v[112:115], v[132:135]\n\t"
      "ds_read_b128 a[108:111], v47 offset:40960\n\t"
      "v_exp_f32 v101, v101\n\t"
      "v_mfma_f32_16x16x32_bf16 v[164:167], a[68:71], v[120:123], v[164:167]\n\t"
      "ds_read_b64_tr_b16 a[134:135], v37 offset:36864\n\t"
      "v_add_f32 v28, v28, v100\n\t"
      "v_mfma_f32_16x16x32_bf16 v[136:139], a[72:75], v[112:115], v[136:139]\n\t"
      "ds_read_b128 a[112:115], v44 offset:45056\n\t"
      "v_exp_f32 v102, v102\n\t"
      "v_mfma_f32_16x16x32_bf16 v[168:171], a[72:75], v[120:123], v[168:171]\n\t"
      "ds_read_b64_tr_b16 a[136:137], v38 offset:32768\n\t"
      "v_add_f32 v28, v28, v101\n\t"
      "v_cvt_pk_bf16_f32 v98, v100, v101\n\t"
      "v_mfma_f32_16x16x32_bf16 v[140:143], a[76:79], v[112:115], v[140:143]\n\t"
      "ds_read_b128 a[116:119], v45 offset:45056\n\t"
      "v_exp_f32 v103, v103\n\t"
      "v_mfma_f32_16x16x32_bf16 v[172:175], a[76:79], v[120:123], v[172:175]\n\t"
      "ds_read_b64_tr_b16 a[138:139], v38 offset:36864\n\t"
      "v_add_f32 v28, v28, v102\n\t"
      "v_mfma_f32_16x16x32_bf16 v[144:147], a[80:83], v[112:115], v[144:147]\n\t"
      "ds_read_b128 a[120:123], v46 offset:45056\n\t"
      "v_exp_f32 v104, v104\n\t"
      "v_mfma_f32_16x16x32_bf16 v[176:179], a[80:83], v[120:123], v[176:179]\n\t"
      "ds_read_b64_tr_b16 a[140:141], v39 offset:32768\n\t"
      "v_add_f32 v28, v28, v103\n\t"
      "v_cvt_pk_bf16_f32 v99, v102, v103\n\t"
      "s_waitcnt lgkmcnt(4)\n\t"
      "v_mfma_f32_16x16x32_bf16 v[148:151], a[84:87], v[112:115], v[148:151]\n\t"
      "ds_read_b128 a[124:127], v47 offset:45056\n\t"
      "v_exp_f32 v105, v105\n\t"
      "v_mfma_f32_16x16x32_bf16 v[180:183], a[84:87], v[120:123], v[180:183]\n\t"
      "ds_read_b64_tr_b16 a[142:143], v39 offset:36864\n\t"
      "v_add_f32 v29, v29, v104\n\t"
      "v_mfma_f32_16x16x32_bf16 v[152:155], a[88:91], v[112:115], v[152:155]\n\t"
      "v_exp_f32 v106, v106\n\t"
      "v_mfma_f32_16x16x32_bf16 v[184:187], a[88:91], v[120:123], v[184:187]\n\t"
      "ds_read_b64_tr_b16 a[144:145], v40 offset:32768\n\t"
      "v_add_f32 v29, v29, v105\n\t"
      "v_cvt_pk_bf16_f32 v104, v104, v105\n\t"
      "v_mfma_f32_16x16x32_bf16 v[156:159], a[92:95], v[112:115], v[156:159]\n\t"
      "v_exp_f32 v107, v107\n\t"
      "v_mfma_f32_16x16x32_bf16 v[188:191], a[92:95], v[120:123], v[188:191]\n\t"
      "ds_read_b64_tr_b16 a[146:147], v40 offset:36864\n\t"
      "v_add_f32 v29, v29, v106\n\t"
      "v_mfma_f32_16x16x32_bf16 v[112:115], a[96:99], a[0:3], v[80:83]\n\t"
      "v_exp_f32 v108, v108\n\t"
      "v_mfma_f32_16x16x32_bf16 v[112:115], a[100:103], a[4:7], v[112:115]\n\t"
      "ds_read_b64_tr_b16 a[148:149], v41 offset:32768\n\t"
      "v_add_f32 v29, v29, v107\n\t"
      "v_cvt_pk_bf16_f32 v105, v106, v107\n\t"
      "v_mfma_f32_16x16x32_bf16 v[112:115], a[104:107], a[8:11], v[112:115]\n\t"
      "v_exp_f32 v109, v109\n\t"
      "v_mfma_f32_16x16x32_bf16 v[112:115], a[108:111], a[12:15], v[112:115]\n\t"
      "ds_read_b64_tr_b16 a[150:151], v41 offset:36864\n\t"
      "v_add_f32 v29, v29, v108\n\t"
      "s_waitcnt lgkmcnt(2)\n\t"
      "v_mfma_f32_16x16x32_bf16 v[116:119], a[112:115], a[0:3], v[80:83]\n\t"
      "v_exp_f32 v110, v110\n\t"
      "v_mfma_f32_16x16x32_bf16 v[116:119], a[116:119], a[4:7], v[116:119]\n\t"
      "ds_read_b64_tr_b16 a[152:153], v42 offset:32768\n\t"
      "v_add_f32 v29, v29, v109\n\t"
      "v_cvt_pk_bf16_f32 v106, v108, v109\n\t"
      "v_mfma_f32_16x16x32_bf16 v[116:119], a[120:123], a[8:11], v[116:119]\n\t"
      "v_exp_f32 v111, v111\n\t"
      "v_mfma_f32_16x16x32_bf16 v[116:119], a[124:127], a[12:15], v[116:119]\n\t"
      "ds_read_b64_tr_b16 a[154:155], v42 offset:36864\n\t"
      "v_add_f32 v29, v29, v110\n\t"
      "v_mfma_f32_16x16x32_bf16 v[120:123], a[96:99], a[16:19], v[84:87]\n\t"
      "v_exp_f32 v112, v112\n\t"
      "v_mfma_f32_16x16x32_bf16 v[120:123], a[100:103], a[20:23], v[120:123]\n\t"
      "ds_read_b64_tr_b16 a[156:157], v43 offset:32768\n\t"
      "v_add_f32 v29, v29, v111\n\t"
      "v_cvt_pk_bf16_f32 v107, v110, v111\n\t"
      "v_mfma_f32_16x16x32_bf16 v[120:123], a[104:107], a[24:27], v[120:123]\n\t"
      "v_exp_f32 v113, v113\n\t"
      "v_mfma_f32_16x16x32_bf16 v[120:123], a[108:111], a[28:31], v[120:123]\n\t"
      "ds_read_b64_tr_b16 a[158:159], v43 offset:36864\n\t"
      "v_add_f32 v28, v28, v112\n\t"
      "v_mfma_f32_16x16x32_bf16 v[124:127], a[112:115], a[16:19], v[84:87]\n\t"
      "v_exp_f32 v114, v114\n\t"
      "v_mfma_f32_16x16x32_bf16 v[124:127], a[116:119], a[20:23], v[124:127]\n\t"
      "ds_read_b128 a[32:35], v44 offset:0\n\t"
      "v_add_f32 v28, v28, v113\n\t"
      "v_cvt_pk_bf16_f32 v112, v112, v113\n\t"
      "v_mfma_f32_16x16x32_bf16 v[124:127], a[120:123], a[24:27], v[124:127]\n\t"
      "v_exp_f32 v115, v115\n\t"
      "v_mfma_f32_16x16x32_bf16 v[124:127], a[124:127], a[28:31], v[124:127]\n\t"
      "ds_read_b128 a[36:39], v45 offset:0\n\t"
      "v_add_f32 v28, v28, v114\n\t"
      "v_mfma_f32_16x16x32_bf16 v[128:131], a[128:131], v[96:99], v[128:131]\n\t"
      "v_exp_f32 v116, v116\n\t"
      "v_mfma_f32_16x16x32_bf16 v[160:163], a[128:131], v[104:107], v[160:163]\n\t"
      "ds_read_b128 a[40:43], v46 offset:0\n\t"
      "v_add_f32 v28, v28, v115\n\t"
      "v_cvt_pk_bf16_f32 v113, v114, v115\n\t"
      "v_mfma_f32_16x16x32_bf16 v[132:135], a[132:135], v[96:99], v[132:135]\n\t"
      "v_exp_f32 v117, v117\n\t"
      "v_mfma_f32_16x16x32_bf16 v[164:167], a[132:135], v[104:107], v[164:167]\n\t"
      "ds_read_b128 a[44:47], v47 offset:0\n\t"
      "v_add_f32 v28, v28, v116\n\t"
      "v_mfma_f32_16x16x32_bf16 v[136:139], a[136:139], v[96:99], v[136:139]\n\t"
      "v_exp_f32 v118, v118\n\t"
      "v_mfma_f32_16x16x32_bf16 v[168:171], a[136:139], v[104:107], v[168:171]\n\t"
      "ds_read_b128 a[48:51], v44 offset:4096\n\t"
      "v_add_f32 v28, v28, v117\n\t"
      "v_cvt_pk_bf16_f32 v114, v116, v117\n\t"
      "v_mfma_f32_16x16x32_bf16 v[140:143], a[140:143], v[96:99], v[140:143]\n\t"
      "s_waitcnt vmcnt(0)\n\t"
      "s_barrier\n\t"
      "v_exp_f32 v119, v119\n\t"
      "v_mfma_f32_16x16x32_bf16 v[172:175], a[140:143], v[104:107], v[172:175]\n\t"
      "ds_read_b128 a[52:55], v45 offset:4096\n\t"
      "v_add_f32 v28, v28, v118\n\t"
      "v_mfma_f32_16x16x32_bf16 v[144:147], a[144:147], v[96:99], v[144:147]\n\t"
      "ds_read_b128 a[56:59], v46 offset:4096\n\t"
      "v_exp_f32 v120, v120\n\t"
      "v_mfma_f32_16x16x32_bf16 v[176:179], a[144:147], v[104:107], v[176:179]\n\t"
      "ds_read_b64_tr_b16 a[64:65], v36 offset:40960\n\t"
      "ds_read_b64_tr_b16 a[66:67], v36 offset:45056\n\t"
      "v_add_f32 v28, v28, v119\n\t"
      "v_cvt_pk_bf16_f32 v115, v118, v119\n\t"
      "s_waitcnt lgkmcnt(4)\n\t"
      "v_mfma_f32_16x16x32_bf16 v[148:151], a[148:151], v[96:99], v[148:151]\n\t"
      "ds_read_b128 a[60:63], v47 offset:4096\n\t"
      "v_exp_f32 v121, v121\n\t"
      "v_mfma_f32_16x16x32_bf16 v[180:183], a[148:151], v[104:107], v[180:183]\n\t"
      "ds_read_b64_tr_b16 a[68:69], v37 offset:40960\n\t"
      "ds_read_b64_tr_b16 a[70:71], v37 offset:45056\n\t"
      "v_add_f32 v29, v29, v120\n\t"
      "v_mfma_f32_16x16x32_bf16 v[152:155], a[152:155], v[96:99], v[152:155]\n\t"
      "ds_read_b64_tr_b16 a[72:73], v38 offset:40960\n\t"
      "v_exp_f32 v122, v122\n\t"
      "v_mfma_f32_16x16x32_bf16 v[184:187], a[152:155], v[104:107], v[184:187]\n\t"
      "ds_read_b64_tr_b16 a[74:75], v38 offset:45056\n\t"
      "v_add_f32 v29, v29, v121\n\t"
      "v_cvt_pk_bf16_f32 v120, v120, v121\n\t"
      "v_mfma_f32_16x16x32_bf16 v[156:159], a[156:159], v[96:99], v[156:159]\n\t"
      "ds_read_b64_tr_b16 a[76:77], v39 offset:40960\n\t"
      "v_exp_f32 v123, v123\n\t"
      "v_mfma_f32_16x16x32_bf16 v[188:191], a[156:159], v[104:107], v[188:191]\n\t"
      "ds_read_b64_tr_b16 a[78:79], v39 offset:45056\n\t"
      "v_add_f32 v29, v29, v122\n\t"
      "v_mfma_f32_16x16x32_bf16 v[96:99], a[32:35], v[48:51], v[88:91]\n\t"
      "ds_read_b64_tr_b16 a[80:81], v40 offset:40960\n\t"
      "v_exp_f32 v124, v124\n\t"
      "v_mfma_f32_16x16x32_bf16 v[96:99], a[36:39], v[52:55], v[96:99]\n\t"
      "ds_read_b64_tr_b16 a[82:83], v40 offset:45056\n\t"
      "v_add_f32 v29, v29, v123\n\t"
      "v_cvt_pk_bf16_f32 v121, v122, v123\n\t"
      "v_mfma_f32_16x16x32_bf16 v[96:99], a[40:43], v[56:59], v[96:99]\n\t"
      "ds_read_b64_tr_b16 a[84:85], v41 offset:40960\n\t"
      "v_exp_f32 v125, v125\n\t"
      "v_mfma_f32_16x16x32_bf16 v[96:99], a[44:47], v[60:63], v[96:99]\n\t"
      "ds_read_b64_tr_b16 a[86:87], v41 offset:45056\n\t"
      "v_add_f32 v29, v29, v124\n\t"
      "s_waitcnt lgkmcnt(4)\n\t"
      "v_mfma_f32_16x16x32_bf16 v[100:103], a[48:51], v[48:51], v[88:91]\n\t"
      "ds_read_b64_tr_b16 a[88:89], v42 offset:40960\n\t"
      "v_exp_f32 v126, v126\n\t"
      "v_mfma_f32_16x16x32_bf16 v[100:103], a[52:55], v[52:55], v[100:103]\n\t"
      "ds_read_b64_tr_b16 a[90:91], v42 offset:45056\n\t"
      "v_add_f32 v29, v29, v125\n\t"
      "v_cvt_pk_bf16_f32 v122, v124, v125\n\t"
      "v_mfma_f32_16x16x32_bf16 v[100:103], a[56:59], v[56:59], v[100:103]\n\t"
      "ds_read_b64_tr_b16 a[92:93], v43 offset:40960\n\t"
      "v_exp_f32 v127, v127\n\t"
      "v_mfma_f32_16x16x32_bf16 v[100:103], a[60:63], v[60:63], v[100:103]\n\t"
      "ds_read_b64_tr_b16 a[94:95], v43 offset:45056\n\t"
      "v_add_f32 v29, v29, v126\n\t"
      "v_mfma_f32_16x16x32_bf16 v[104:107], a[32:35], v[64:67], v[92:95]\n\t"
      "v_exp_f32 v96, v96\n\t"
      "v_mfma_f32_16x16x32_bf16 v[104:107], a[36:39], v[68:71], v[104:107]\n\t"
      "v_add_f32 v29, v29, v127\n\t"
      "v_cvt_pk_bf16_f32 v123, v126, v127\n\t"
      "v_mfma_f32_16x16x32_bf16 v[104:107], a[40:43], v[72:75], v[104:107]\n\t"
      "v_exp_f32 v97, v97\n\t"
      "v_mfma_f32_16x16x32_bf16 v[104:107], a[44:47], v[76:79], v[104:107]\n\t"
      "v_add_f32 v30, v30, v96\n\t"
      "v_mfma_f32_16x16x32_bf16 v[108:111], a[48:51], v[64:67], v[92:95]\n\t"
      "ds_read_b128 a[96:99], v44 offset:8192\n\t"
      "v_exp_f32 v98, v98\n\t"
      "v_mfma_f32_16x16x32_bf16 v[108:111], a[52:55], v[68:71], v[108:111]\n\t"
      "ds_read_b64_tr_b16 a[128:129], v36 offset:0\n\t"
      "v_add_f32 v30, v30, v97\n\t"
      "v_cvt_pk_bf16_f32 v96, v96, v97\n\t"
      "v_mov_b32 v4, v28\n\t"
      "v_mov_b32 v6, v29\n\t"
      "s_nop 1\n\t"
      "v_permlane16_swap_b32 v4, v28\n\t"
      "v_permlane16_swap_b32 v6, v29\n\t"
      "s_nop 1\n\t"
      "v_add_f32 v28, v28, v4\n\t"
      "v_add_f32 v29, v29, v6\n\t"
      "v_mov_b32 v4, v28\n\t"
      "v_mfma_f32_16x16x32_bf16 v[108:111], a[56:59], v[72:75], v[108:111]\n\t"
      "ds_read_b128 a[100:103], v45 offset:8192\n\t"
      "v_exp_f32 v99, v99\n\t"
      "v_mfma_f32_16x16x32_bf16 v[108:111], a[60:63], v[76:79], v[108:111]\n\t"
      "ds_read_b64_tr_b16 a[130:131], v36 offset:4096\n\t"
      "v_add_f32 v30, v30, v98\n\t"
      "v_mov_b32 v6, v29\n\t"
      "s_nop 1\n\t"
      "v_permlane32_swap_b32 v4, v28\n\t"
      "v_permlane32_swap_b32 v6, v29\n\t"
      "s_nop 1\n\t"
      "v_add_f32 v28, v28, v4\n\t"
      "v_add_f32 v29, v29, v6\n\t"
      "v_cmp_nlt_f32_e64 vcc, v28, s51\n\t"
      "v_cmp_nlt_f32_e64 s[42:43], v29, s51\n\t"
      "v_rcp_f32 v4, v28\n\t"
      "v_rcp_f32 v6, v29\n\t"
      "s_or_b64 s[52:53], s[52:53], vcc\n\t"
      "s_or_b64 s[52:53], s[52:53], s[42:43]\n\t"
      "v_mfma_f32_16x16x32_bf16 v[128:131], a[64:67], v[112:115], v[128:131]\n\t"
      "ds_read_b128 a[104:107], v46 offset:8192\n\t"
      "v_exp_f32 v100, v100\n\t"
      "v_mfma_f32_16x16x32_bf16 v[160:163], a[64:67], v[120:123], v[160:163]\n\t"
      "ds_read_b64_tr_b16 a[132:133], v37 offset:0\n\t"
      "v_add_f32 v30, v30, v99\n\t"
      "v_cvt_pk_bf16_f32 v97, v98, v99\n\t"
      "v_mfma_f32_16x16x32_bf16 v[132:135], a[68:71], v[112:115], v[132:135]\n\t"
      "ds_read_b128 a[108:111], v47 offset:8192\n\t"
      "v_exp_f32 v101, v101\n\t"
      "v_mfma_f32_16x16x32_bf16 v[164:167], a[68:71], v[120:123], v[164:167]\n\t"
      "ds_read_b64_tr_b16 a[134:135], v37 offset:4096\n\t"
      "v_add_f32 v30, v30, v100\n\t"
      "v_mfma_f32_16x16x32_bf16 v[136:139], a[72:75], v[112:115], v[136:139]\n\t"
      "ds_read_b128 a[112:115], v44 offset:12288\n\t"
      "v_exp_f32 v102, v102\n\t"
      "v_mfma_f32_16x16x32_bf16 v[168:171], a[72:75], v[120:123], v[168:171]\n\t"
      "ds_read_b64_tr_b16 a[136:137], v38 offset:0\n\t"
      "v_add_f32 v30, v30, v101\n\t"
      "v_cvt_pk_bf16_f32 v98, v100, v101\n\t"
      "v_mfma_f32_16x16x32_bf16 v[140:143], a[76:79], v[112:115], v[140:143]\n\t"
      "ds_read_b128 a[116:119], v45 offset:12288\n\t"
      "v_exp_f32 v103, v103\n\t"
      "v_mfma_f32_16x16x32_bf16 v[172:175], a[76:79], v[120:123], v[172:175]\n\t"
      "ds_read_b64_tr_b16 a[138:139], v38 offset:4096\n\t"
      "v_add_f32 v30, v30, v102\n\t"
      "s_waitcnt lgkmcnt(4)\n\t"
      "v_mfma_f32_16x16x32_bf16 v[144:147], a[80:83], v[112:115], v[144:147]\n\t"
      "ds_read_b128 a[120:123], v46 offset:12288\n\t"
      "v_exp_f32 v104, v104\n\t"
      "v_mfma_f32_16x16x32_bf16 v[176:179], a[80:83], v[120:123], v[176:179]\n\t"
      "ds_read_b64_tr_b16 a[140:141], v39 offset:0\n\t"
      "v_add_f32 v30, v30, v103\n\t"
      "v_cvt_pk_bf16_f32 v99, v102, v103\n\t"
      "v_mfma_f32_16x16x32_bf16 v[148:151], a[84:87], v[112:115], v[148:151]\n\t"
      "ds_read_b128 a[124:127], v47 offset:12288\n\t"
      "v_exp_f32 v105, v105\n\t"
      "v_mfma_f32_16x16x32_bf16 v[180:183], a[84:87], v[120:123], v[180:183]\n\t"
      "ds_read_b64_tr_b16 a[142:143], v39 offset:4096\n\t"
      "v_add_f32 v31, v31, v104\n\t"
      "v_mfma_f32_16x16x32_bf16 v[152:155], a[88:91], v[112:115], v[152:155]\n\t"
      "v_exp_f32 v106, v106\n\t"
      "v_mfma_f32_16x16x32_bf16 v[184:187], a[88:91], v[120:123], v[184:187]\n\t"
      "ds_read_b64_tr_b16 a[144:145], v40 offset:0\n\t"
      "v_add_f32 v31, v31, v105\n\t"
      "v_cvt_pk_bf16_f32 v104, v104, v105\n\t"
      "v_mfma_f32_16x16x32_bf16 v[156:159], a[92:95], v[112:115], v[156:159]\n\t"
      "v_exp_f32 v107, v107\n\t"
      "v_mfma_f32_16x16x32_bf16 v[188:191], a[92:95], v[120:123], v[188:191]\n\t"
      "ds_read_b64_tr_b16 a[146:147], v40 offset:4096\n\t"
      "v_add_f32 v31, v31, v106\n\t"
      "v_mfma_f32_16x16x32_bf16 v[112:115], a[96:99], v[48:51], v[88:91]\n\t"
      "v_exp_f32 v108, v108\n\t"
      "v_mfma_f32_16x16x32_bf16 v[112:115], a[100:103], v[52:55], v[112:115]\n\t"
      "ds_read_b64_tr_b16 a[148:149], v41 offset:0\n\t"
      "v_add_f32 v31, v31, v107\n\t"
      "v_cvt_pk_bf16_f32 v105, v106, v107\n\t"
      "v_mfma_f32_16x16x32_bf16 v[112:115], a[104:107], v[56:59], v[112:115]\n\t"
      "v_exp_f32 v109, v109\n\t"
      "v_mfma_f32_16x16x32_bf16 v[112:115], a[108:111], v[60:63], v[112:115]\n\t"
      "ds_read_b64_tr_b16 a[150:151], v41 offset:4096\n\t"
      "v_add_f32 v31, v31, v108\n\t"
      "v_mul_f32 v128, v128, v4\n\t"
      "v_mul_f32 v129, v129, v4\n\t"
      "v_mul_f32 v130, v130, v4\n\t"
      "v_mul_f32 v131, v131, v4\n\t"
      "s_waitcnt lgkmcnt(2)\n\t"
      "v_mfma_f32_16x16x32_bf16 v[116:119], a[112:115], v[48:51], v[88:91]\n\t"
      "v_exp_f32 v110, v110\n\t"
      "v_mfma_f32_16x16x32_bf16 v[116:119], a[116:119], v[52:55], v[116:119]\n\t"
      "ds_read_b64_tr_b16 a[152:153], v42 offset:0\n\t"
      "v_add_f32 v31, v31, v109\n\t"
      "v_cvt_pk_bf16_f32 v106, v108, v109\n\t"
      "v_cvt_pk_bf16_f32 v128, v128, v129\n\t"
      "v_cvt_pk_bf16_f32 v129, v130, v131\n\t"
      "v_mul_f32 v132, v132, v4\n\t"
      "v_mul_f32 v133, v133, v4\n\t"
      "v_mul_f32 v134, v134, v4\n\t"
      "v_mul_f32 v135, v135, v4\n\t"
      "v_mfma_f32_16x16x32_bf16 v[116:119], a[120:123], v[56:59], v[116:119]\n\t"
      "ds_read_b64_tr_b16 a[154:155], v42 offset:4096\n\t"
      "v_exp_f32 v111, v111\n\t"
      "v_mfma_f32_16x16x32_bf16 v[116:119], a[124:127], v[60:63], v[116:119]\n\t"
      "ds_write_b64 v8, v[128:129] offset:0\n\t"
      "v_add_f32 v31, v31, v110\n\t"
      "v_cvt_pk_bf16_f32 v132, v132, v133\n\t"
      "v_cvt_pk_bf16_f32 v133, v134, v135\n\t"
      "v_mul_f32 v136, v136, v4\n\t"
      "v_mul_f32 v137, v137, v4\n\t"
      "v_mul_f32 v138, v138, v4\n\t"
      "v_mul_f32 v139, v139, v4\n\t"
      "v_mfma_f32_16x16x32_bf16 v[120:123], a[96:99], v[64:67], v[92:95]\n\t"
      "ds_read_b64_tr_b16 a[156:157], v43 offset:0\n\t"
      "v_exp_f32 v112, v112\n\t"
      "v_mfma_f32_16x16x32_bf16 v[120:123], a[100:103], v[68:71], v[120:123]\n\t"
      "ds_write_b64 v9, v[132:133] offset:0\n\t"
      "v_add_f32 v31, v31, v111\n\t"
      "v_cvt_pk_bf16_f32 v107, v110, v111\n\t"
      "v_cvt_pk_bf16_f32 v136, v136, v137\n\t"
      "v_cvt_pk_bf16_f32 v137, v138, v139\n\t"
      "v_mul_f32 v140, v140, v4\n\t"
      "v_mul_f32 v141, v141, v4\n\t"
      "v_mul_f32 v142, v142, v4\n\t"
      "v_mul_f32 v143, v143, v4\n\t"
      "v_mfma_f32_16x16x32_bf16 v[120:123], a[104:107], v[72:75], v[120:123]\n\t"
      "ds_read_b64_tr_b16 a[158:159], v43 offset:4096\n\t"
      "v_exp_f32 v113, v113\n\t"
      "v_mfma_f32_16x16x32_bf16 v[120:123], a[108:111], v[76:79], v[120:123]\n\t"
      "ds_write_b64 v10, v[136:137] offset:0\n\t"
      "v_add_f32 v30, v30, v112\n\t"
      "v_cvt_pk_bf16_f32 v140, v140, v141\n\t"
      "v_cvt_pk_bf16_f32 v141, v142, v143\n\t"
      "v_mul_f32 v144, v144, v4\n\t"
      "v_mul_f32 v145, v145, v4\n\t"
      "v_mul_f32 v146, v146, v4\n\t"
      "v_mul_f32 v147, v147, v4\n\t"
      "v_mfma_f32_16x16x32_bf16 v[124:127], a[112:115], v[64:67], v[92:95]\n\t"
      "ds_read_b128 a[32:35], v44 offset:32768\n\t"
      "v_exp_f32 v114, v114\n\t"
      "v_mfma_f32_16x16x32_bf16 v[124:127], a[116:119], v[68:71], v[124:127]\n\t"
      "ds_read_b64_tr_b16 a[64:65], v36 offset:8192\n\t"
      "ds_write_b64 v11, v[140:141] offset:0\n\t"
      "v_add_f32 v30, v30, v113\n\t"
      "v_cvt_pk_bf16_f32 v112, v112, v113\n\t"
      "v_cvt_pk_bf16_f32 v144, v144, v145\n\t"
      "v_cvt_pk_bf16_f32 v145, v146, v147\n\t"
      "v_mul_f32 v148, v148, v4\n\t"
      "v_mul_f32 v149, v149, v4\n\t"
      "v_mul_f32 v150, v150, v4\n\t"
      "v_mul_f32 v151, v151, v4\n\t"
      "v_mfma_f32_16x16x32_bf16 v[124:127], a[120:123], v[72:75], v[124:127]\n\t"
      "ds_read_b128 a[36:39], v45 offset:32768\n\t"
      "v_exp_f32 v115, v115\n\t"
      "v_mfma_f32_16x16x32_bf16 v[124:127], a[124:127], v[76:79], v[124:127]\n\t"
      "ds_read_b64_tr_b16 a[66:67], v36 offset:12288\n\t"
      "ds_write_b64 v12, v[144:145] offset:0\n\t"
      "v_add_f32 v30, v30, v114\n\t"
      "v_cvt_pk_bf16_f32 v148, v148, v149\n\t"
      "v_cvt_pk_bf16_f32 v149, v150, v151\n\t"
      "v_mul_f32 v152, v152, v4\n\t"
      "v_mul_f32 v153, v153, v4\n\t"
      "v_mul_f32 v154, v154, v4\n\t"
      "v_mul_f32 v155, v155, v4\n\t"
      "v_mfma_f32_16x16x32_bf16 v[192:195], a[128:131], v[96:99], v[192:195]\n\t"
      "ds_read_b128 a[40:43], v46 offset:32768\n\t"
      "v_exp_f32 v116, v116\n\t"
      "v_mfma_f32_16x16x32_bf16 v[224:227], a[128:131], v[104:107], v[224:227]\n\t"
      "ds_read_b64_tr_b16 a[68:69], v37 offset:8192\n\t"
      "ds_write_b64 v13, v[148:149] offset:0\n\t"
      "v_add_f32 v30, v30, v115\n\t"
      "v_cvt_pk_bf16_f32 v113, v114, v115\n\t"
      "v_cvt_pk_bf16_f32 v152, v152, v153\n\t"
      "v_cvt_pk_bf16_f32 v153, v154, v155\n\t"
      "v_mul_f32 v156, v156, v4\n\t"
      "v_mul_f32 v157, v157, v4\n\t"
      "v_mul_f32 v158, v158, v4\n\t"
      "v_mul_f32 v159, v159, v4\n\t"
      "v_mfma_f32_16x16x32_bf16 v[196:199], a[132:135], v[96:99], v[196:199]\n\t"
      "ds_read_b128 a[44:47], v47 offset:32768\n\t"
      "v_exp_f32 v117, v117\n\t"
      "v_mfma_f32_16x16x32_bf16 v[228:231], a[132:135], v[104:107], v[228:231]\n\t"
      "ds_read_b64_tr_b16 a[70:71], v37 offset:12288\n\t"
      "ds_write_b64 v14, v[152:153] offset:0\n\t"
      "v_add_f32 v30, v30, v116\n\t"
      "v_cvt_pk_bf16_f32 v156, v156, v157\n\t"
      "v_cvt_pk_bf16_f32 v157, v158, v159\n\t"
      "v_mul_f32 v160, v160, v6\n\t"
      "v_mul_f32 v161, v161, v6\n\t"
      "v_mul_f32 v162, v162, v6\n\t"
      "v_mul_f32 v163, v163, v6\n\t"
      "v_mfma_f32_16x16x32_bf16 v[200:203], a[136:139], v[96:99], v[200:203]\n\t"
      "ds_read_b128 a[48:51], v44 offset:36864\n\t"
      "v_exp_f32 v118, v118\n\t"
      "v_mfma_f32_16x16x32_bf16 v[232:235], a[136:139], v[104:107], v[232:235]\n\t"
      "ds_read_b64_tr_b16 a[72:73], v38 offset:8192\n\t"
      "ds_write_b64 v15, v[156:157] offset:0\n\t"
      "v_add_f32 v30, v30, v117\n\t"
      "v_cvt_pk_bf16_f32 v114, v116, v117\n\t"
      "v_cvt_pk_bf16_f32 v160, v160, v161\n\t"
      "v_cvt_pk_bf16_f32 v161, v162, v163\n\t"
      "v_mul_f32 v164, v164, v6\n\t"
      "v_mul_f32 v165, v165, v6\n\t"
      "v_mul_f32 v166, v166, v6\n\t"
      "v_mul_f32 v167, v167, v6\n\t"
      "v_mfma_f32_16x16x32_bf16 v[204:207], a[140:143], v[96:99], v[204:207]\n\t"
      "ds_read_b128 a[52:55], v45 offset:36864\n\t"
      "v_exp_f32 v119, v119\n\t"
      "v_mfma_f32_16x16x32_bf16 v[236:239], a[140:143], v[104:107], v[236:239]\n\t"
      "ds_read_b64_tr_b16 a[74:75], v38 offset:12288\n\t"
      "ds_read_b128 v[128:131], v16 offset:0\n\t"
      "ds_write_b64 v8, v[160:161] offset:4096\n\t"
      "v_add_f32 v30, v30, v118\n\t"
      "v_cvt_pk_bf16_f32 v164, v164, v165\n\t"
      "v_cvt_pk_bf16_f32 v165, v166, v167\n\t"
      "v_mul_f32 v168, v168, v6\n\t"
      "v_mul_f32 v169, v169, v6\n\t"
      "v_mul_f32 v170, v170, v6\n\t"
      "v_mul_f32 v171, v171, v6\n\t"
      "v_mfma_f32_16x16x32_bf16 v[208:211], a[144:147], v[96:99], v[208:211]\n\t"
      "ds_read_b128 a[56:59], v46 offset:36864\n\t"
      "v_exp_f32 v120, v120\n\t"
      "v_mfma_f32_16x16x32_bf16 v[240:243], a[144:147], v[104:107], v[240:243]\n\t"
      "ds_read_b64_tr_b16 a[76:77], v39 offset:8192\n\t"
      "ds_read_b128 v[132:135], v17 offset:1024\n\t"
      "ds_write_b64 v9, v[164:165] offset:4096\n\t"
      "v_add_f32 v30, v30, v119\n\t"
      "v_cvt_pk_bf16_f32 v115, v118, v119\n\t"
      "v_cvt_pk_bf16_f32 v168, v168, v169\n\t"
      "v_cvt_pk_bf16_f32 v169, v170, v171\n\t"
      "v_mul_f32 v172, v172, v6\n\t"
      "v_mul_f32 v173, v173, v6\n\t"
      "v_mul_f32 v174, v174, v6\n\t"
      "v_mul_f32 v175, v175, v6\n\t"
      "s_waitcnt lgkmcnt(8)\n\t"
      "v_mfma_f32_16x16x32_bf16 v[212:215], a[148:151], v[96:99], v[212:215]\n\t"
      "ds_read_b128 a[60:63], v47 offset:36864\n\t"
      "v_exp_f32 v121, v121\n\t"
      "v_mfma_f32_16x16x32_bf16 v[244:247], a[148:151], v[104:107], v[244:247]\n\t"
      "ds_read_b64_tr_b16 a[78:79], v39 offset:12288\n\t"
      "ds_read_b128 v[136:139], v16 offset:2048\n\t"
      "ds_write_b64 v10, v[168:169] offset:4096\n\t"
      "v_add_f32 v31, v31, v120\n\t"
      "v_cvt_pk_bf16_f32 v172, v172, v173\n\t"
      "v_cvt_pk_bf16_f32 v173, v174, v175\n\t"
      "v_mul_f32 v176, v176, v6\n\t"
      "v_mul_f32 v177, v177, v6\n\t"
      "v_mul_f32 v178, v178, v6\n\t"
      "v_mul_f32 v179, v179, v6\n\t"
      "v_mfma_f32_16x16x32_bf16 v[216:219], a[152:155], v[96:99], v[216:219]\n\t"
      "ds_read_b64_tr_b16 a[80:81], v40 offset:8192\n\t"
      "v_exp_f32 v122, v122\n\t"
      "v_mfma_f32_16x16x32_bf16 v[248:251], a[152:155], v[104:107], v[248:251]\n\t"
      "ds_read_b128 v[140:143], v17 offset:3072\n\t"
      "ds_write_b64 v11, v[172:173] offset:4096\n\t"
      "s_waitcnt lgkmcnt(7)\n\t"
      "s_add_u32 s45, s55, 0\n\t"
      "buffer_store_dwordx4 v[128:131], v18, s[56:59], s45 offen offset:0 nt\n\t"
      "v_add_f32 v31, v31, v121\n\t"
      "v_cvt_pk_bf16_f32 v120, v120, v121\n\t"
      "v_cvt_pk_bf16_f32 v176, v176, v177\n\t"
      "v_cvt_pk_bf16_f32 v177, v178, v179\n\t"
      "v_mul_f32 v180, v180, v6\n\t"
      "v_mul_f32 v181, v181, v6\n\t"
      "v_mul_f32 v182, v182, v6\n\t"
      "v_mul_f32 v183, v183, v6\n\t"
      "v_mfma_f32_16x16x32_bf16 v[220:223], a[156:159], v[96:99], v[220:223]\n\t"
      "ds_read_b64_tr_b16 a[82:83], v40 offset:12288\n\t"
      "v_exp_f32 v123, v123\n\t"
      "v_mfma_f32_16x16x32_bf16 v[252:255], a[156:159], v[104:107], v[252:255]\n\t"
      "ds_write_b64 v12, v[176:177] offset:4096\n\t"
      "buffer_store_dwordx4 v[132:135], v18, s[56:59], s45 offen offset:1024 nt\n\t"
      "v_add_f32 v31, v31, v122\n\t"
      "v_cvt_pk_bf16_f32 v180, v180, v181\n\t"
      "v_cvt_pk_bf16_f32 v181, v182, v183\n\t"
      "v_mul_f32 v184, v184, v6\n\t"
      "v_mul_f32 v185, v185, v6\n\t"
      "v_mul_f32 v186, v186, v6\n\t"
      "v_mul_f32 v187, v187, v6\n\t"
      "v_mfma_f32_16x16x32_bf16 v[96:99], a[32:35], v[48:51], v[88:91]\n\t"
      "ds_read_b64_tr_b16 a[84:85], v41 offset:8192\n\t"
      "v_exp_f32 v124, v124\n\t"
      "v_mfma_f32_16x16x32_bf16 v[96:99], a[36:39], v[52:55], v[96:99]\n\t"
      "ds_write_b64 v13, v[180:181] offset:4096\n\t"
      "s_waitcnt lgkmcnt(4)\n\t"
      "buffer_store_dwordx4 v[136:139], v18, s[56:59], s45 offen offset:2048 nt\n\t"
      "v_add_f32 v31, v31, v123\n\t"
      "v_cvt_pk_bf16_f32 v121, v122, v123\n\t"
      "v_cvt_pk_bf16_f32 v184, v184, v185\n\t"
      "v_cvt_pk_bf16_f32 v185, v186, v187\n\t"
      "v_mul_f32 v188, v188, v6\n\t"
      "v_mul_f32 v189, v189, v6\n\t"
      "v_mul_f32 v190, v190, v6\n\t"
      "v_mul_f32 v191, v191, v6\n\t"
      "v_mfma_f32_16x16x32_bf16 v[96:99], a[40:43], v[56:59], v[96:99]\n\t"
      "ds_read_b64_tr_b16 a[86:87], v41 offset:12288\n\t"
      "v_exp_f32 v125, v125\n\t"
      "v_mfma_f32_16x16x32_bf16 v[96:99], a[44:47], v[60:63], v[96:99]\n\t"
      "ds_write_b64 v14, v[184:185] offset:4096\n\t"
      "buffer_store_dwordx4 v[140:143], v18, s[56:59], s45 offen offset:3072 nt\n\t"
      "v_add_f32 v31, v31, v124\n\t"
      "v_cvt_pk_bf16_f32 v188, v188, v189\n\t"
      "v_cvt_pk_bf16_f32 v189, v190, v191\n\t"
      "v_mfma_f32_16x16x32_bf16 v[100:103], a[48:51], v[48:51], v[88:91]\n\t"
      "ds_read_b64_tr_b16 a[88:89], v42 offset:8192\n\t"
      "v_exp_f32 v126, v126\n\t"
      "v_mfma_f32_16x16x32_bf16 v[100:103], a[52:55], v[52:55], v[100:103]\n\t"
      "ds_write_b64 v15, v[188:189] offset:4096\n\t"
      "v_add_f32 v31, v31, v125\n\t"
      "v_cvt_pk_bf16_f32 v122, v124, v125\n\t"
      "v_mfma_f32_16x16x32_bf16 v[100:103], a[56:59], v[56:59], v[100:103]\n\t"
      "ds_read_b64_tr_b16 a[90:91], v42 offset:12288\n\t"
      "v_exp_f32 v127, v127\n\t"
      "v_mfma_f32_16x16x32_bf16 v[100:103], a[60:63], v[60:63], v[100:103]\n\t"
      "ds_read_b128 v[160:163], v16 offset:4096\n\t"
      "v_add_f32 v31, v31, v126\n\t"
      "v_mfma_f32_16x16x32_bf16 v[104:107], a[32:35], v[64:67], v[92:95]\n\t"
      "ds_read_b64_tr_b16 a[92:93], v43 offset:8192\n\t"
      "v_exp_f32 v96, v96\n\t"
      "v_mfma_f32_16x16x32_bf16 v[104:107], a[36:39], v[68:71], v[104:107]\n\t"
      "ds_read_b128 v[164:167], v17 offset:5120\n\t"
      "v_add_f32 v31, v31, v127\n\t"
      "v_cvt_pk_bf16_f32 v123, v126, v127\n\t"
      "v_mfma_f32_16x16x32_bf16 v[104:107], a[40:43], v[72:75], v[104:107]\n\t"
      "ds_read_b64_tr_b16 a[94:95], v43 offset:12288\n\t"
      "v_exp_f32 v97, v97\n\t"
      "v_mfma_f32_16x16x32_bf16 v[104:107], a[44:47], v[76:79], v[104:107]\n\t"
      "ds_read_b128 v[168:171], v16 offset:6144\n\t"
      "v_add_f32 v30, v30, v96\n\t"
      "v_mfma_f32_16x16x32_bf16 v[108:111], a[48:51], v[64:67], v[92:95]\n\t"
      "ds_read_b128 a[96:99], v44 offset:40960\n\t"
      "v_exp_f32 v98, v98\n\t"
      "v_mfma_f32_16x16x32_bf16 v[108:111], a[52:55], v[68:71], v[108:111]\n\t"
      "ds_read_b64_tr_b16 a[128:129], v36 offset:32768\n\t"
      "ds_read_b128 v[172:175], v17 offset:7168\n\t"
      "s_waitcnt lgkmcnt(5)\n\t"
      "s_add_u32 s45, s55, 4096\n\t"
      "buffer_store_dwordx4 v[160:163], v18, s[56:59], s45 offen offset:0 nt\n\t"
      "v_add_f32 v30, v30, v97\n\t"
      "v_cvt_pk_bf16_f32 v96, v96, v97\n\t"
      "v_mfma_f32_16x16x32_bf16 v[108:111], a[56:59], v[72:75], v[108:111]\n\t"
      "ds_read_b128 a[100:103], v45 offset:40960\n\t"
      "v_exp_f32 v99, v99\n\t"
      "v_mfma_f32_16x16x32_bf16 v[108:111], a[60:63], v[76:79], v[108:111]\n\t"
      "ds_read_b64_tr_b16 a[130:131], v36 offset:36864\n\t"
      "buffer_store_dwordx4 v[164:167], v18, s[56:59], s45 offen offset:1024 nt\n\t"
      "v_add_f32 v30, v30, v98\n\t"
      "v_mfma_f32_16x16x32_bf16 v[192:195], a[64:67], v[112:115], v[192:195]\n\t"
      "ds_read_b128 a[104:107], v46 offset:40960\n\t"
      "v_exp_f32 v100, v100\n\t"
      "v_mfma_f32_16x16x32_bf16 v[224:227], a[64:67], v[120:123], v[224:227]\n\t"
      "ds_read_b64_tr_b16 a[132:133], v37 offset:32768\n\t"
      "s_waitcnt lgkmcnt(4)\n\t"
      "buffer_store_dwordx4 v[168:171], v18, s[56:59], s45 offen offset:2048 nt\n\t"
      "v_add_f32 v30, v30, v99\n\t"
      "v_cvt_pk_bf16_f32 v97, v98, v99\n\t"
      "v_mfma_f32_16x16x32_bf16 v[196:199], a[68:71], v[112:115], v[196:199]\n\t"
      "ds_read_b128 a[108:111], v47 offset:40960\n\t"
      "v_exp_f32 v101, v101\n\t"
      "v_mfma_f32_16x16x32_bf16 v[228:231], a[68:71], v[120:123], v[228:231]\n\t"
      "ds_read_b64_tr_b16 a[134:135], v37 offset:36864\n\t"
      "buffer_store_dwordx4 v[172:175], v18, s[56:59], s45 offen offset:3072 nt\n\t"
      "v_add_f32 v30, v30, v100\n\t"
      "v_mfma_f32_16x16x32_bf16 v[200:203], a[72:75], v[112:115], v[200:203]\n\t"
      "ds_read_b128 a[112:115], v44 offset:45056\n\t"
      "v_exp_f32 v102, v102\n\t"
      "v_mfma_f32_16x16x32_bf16 v[232:235], a[72:75], v[120:123], v[232:235]\n\t"
      "ds_read_b64_tr_b16 a[136:137], v38 offset:32768\n\t"
      "v_add_f32 v30, v30, v101\n\t"
      "v_cvt_pk_bf16_f32 v98, v100, v101\n\t"
      "v_mfma_f32_16x16x32_bf16 v[204:207], a[76:79], v[112:115], v[204:207]\n\t"
      "ds_read_b128 a[116:119], v45 offset:45056\n\t"
      "v_exp_f32 v103, v103\n\t"
      "v_mfma_f32_16x16x32_bf16 v[236:239], a[76:79], v[120:123], v[236:239]\n\t"
      "ds_read_b64_tr_b16 a[138:139], v38 offset:36864\n\t"
      "v_add_f32 v30, v30, v102\n\t"
      "v_mfma_f32_16x16x32_bf16 v[208:211], a[80:83], v[112:115], v[208:211]\n\t"
      "ds_read_b128 a[120:123], v46 offset:45056\n\t"
      "v_exp_f32 v104, v104\n\t"
      "v_mfma_f32_16x16x32_bf16 v[240:243], a[80:83], v[120:123], v[240:243]\n\t"
      "ds_read_b64_tr_b16 a[140:141], v39 offset:32768\n\t"
      "v_add_f32 v30, v30, v103\n\t"
      "v_cvt_pk_bf16_f32 v99, v102, v103\n\t"
      "v_mfma_f32_16x16x32_bf16 v[212:215], a[84:87], v[112:115], v[212:215]\n\t"
      "ds_read_b128 a[124:127], v47 offset:45056\n\t"
      "v_exp_f32 v105, v105\n\t"
      "v_mfma_f32_16x16x32_bf16 v[244:247], a[84:87], v[120:123], v[244:247]\n\t"
      "ds_read_b64_tr_b16 a[142:143], v39 offset:36864\n\t"
      "v_add_f32 v31, v31, v104\n\t"
      "v_mfma_f32_16x16x32_bf16 v[216:219], a[88:91], v[112:115], v[216:219]\n\t"
      "v_exp_f32 v106, v106\n\t"
      "v_mfma_f32_16x16x32_bf16 v[248:251], a[88:91], v[120:123], v[248:251]\n\t"
      "ds_read_b64_tr_b16 a[144:145], v40 offset:32768\n\t"
      "v_add_f32 v31, v31, v105\n\t"
      "v_cvt_pk_bf16_f32 v104, v104, v105\n\t"
      "v_mfma_f32_16x16x32_bf16 v[220:223], a[92:95], v[112:115], v[220:223]\n\t"
      "v_exp_f32 v107, v107\n\t"
      "v_mfma_f32_16x16x32_bf16 v[252:255], a[92:95], v[120:123], v[252:255]\n\t"
      "ds_read_b64_tr_b16 a[146:147], v40 offset:36864\n\t"
      "v_add_f32 v31, v31, v106\n\t"
      "s_waitcnt lgkmcnt(2)\n\t"
      "v_mfma_f32_16x16x32_bf16 v[112:115], a[96:99], v[48:51], v[88:91]\n\t"
      "ds_read_b64_tr_b16 a[148:149], v41 offset:32768\n\t"
      "v_exp_f32 v108, v108\n\t"
      "v_mfma_f32_16x16x32_bf16 v[112:115], a[100:103], v[52:55], v[112:115]\n\t"
      "ds_read_b64_tr_b16 a[64:65], v36 offset:40960\n\t"
      "v_add_f32 v31, v31, v107\n\t"
      "v_cvt_pk_bf16_f32 v105, v106, v107\n\t"
      "v_mfma_f32_16x16x32_bf16 v[112:115], a[104:107], v[56:59], v[112:115]\n\t"
      "ds_read_b64_tr_b16 a[150:151], v41 offset:36864\n\t"
      "v_exp_f32 v109, v109\n\t"
      "v_mfma_f32_16x16x32_bf16 v[112:115], a[108:111], v[60:63], v[112:115]\n\t"
      "ds_read_b64_tr_b16 a[66:67], v36 offset:45056\n\t"
      "v_add_f32 v31, v31, v108\n\t"
      "v_mfma_f32_16x16x32_bf16 v[116:119], a[112:115], v[48:51], v[88:91]\n\t"
      "ds_read_b64_tr_b16 a[152:153], v42 offset:32768\n\t"
      "v_exp_f32 v110, v110\n\t"
      "v_mfma_f32_16x16x32_bf16 v[116:119], a[116:119], v[52:55], v[116:119]\n\t"
      "ds_read_b64_tr_b16 a[68:69], v37 offset:40960\n\t"
      "v_add_f32 v31, v31, v109\n\t"
      "v_cvt_pk_bf16_f32 v106, v108, v109\n\t"
      "v_mfma_f32_16x16x32_bf16 v[116:119], a[120:123], v[56:59], v[116:119]\n\t"
      "ds_read_b64_tr_b16 a[154:155], v42 offset:36864\n\t"
      "v_exp_f32 v111, v111\n\t"
      "v_mfma_f32_16x16x32_bf16 v[116:119], a[124:127], v[60:63], v[116:119]\n\t"
      "ds_read_b64_tr_b16 a[70:71], v37 offset:45056\n\t"
      "v_add_f32 v31, v31, v110\n\t"
      "v_mfma_f32_16x16x32_bf16 v[120:123], a[96:99], v[64:67], v[92:95]\n\t"
      "ds_read_b64_tr_b16 a[156:157], v43 offset:32768\n\t"
      "v_exp_f32 v112, v112\n\t"
      "v_mfma_f32_16x16x32_bf16 v[120:123], a[100:103], v[68:71], v[120:123]\n\t"
      "ds_read_b64_tr_b16 a[72:73], v38 offset:40960\n\t"
      "v_add_f32 v31, v31, v111\n\t"
      "v_cvt_pk_bf16_f32 v107, v110, v111\n\t"
      "v_mfma_f32_16x16x32_bf16 v[120:123], a[104:107], v[72:75], v[120:123]\n\t"
      "ds_read_b64_tr_b16 a[158:159], v43 offset:36864\n\t"
      "v_exp_f32 v113, v113\n\t"
      "v_mfma_f32_16x16x32_bf16 v[120:123], a[108:111], v[76:79], v[120:123]\n\t"
      "ds_read_b64_tr_b16 a[74:75], v38 offset:45056\n\t"
      "v_add_f32 v30, v30, v112\n\t"
      "v_mfma_f32_16x16x32_bf16 v[124:127], a[112:115], v[64:67], v[92:95]\n\t"
      "v_exp_f32 v114, v114\n\t"
      "v_mfma_f32_16x16x32_bf16 v[124:127], a[116:119], v[68:71], v[124:127]\n\t"
      "ds_read_b64_tr_b16 a[76:77], v39 offset:40960\n\t"
      "v_add_f32 v30, v30, v113\n\t"
      "v_cvt_pk_bf16_f32 v112, v112, v113\n\t"
      "v_mfma_f32_16x16x32_bf16 v[124:127], a[120:123], v[72:75], v[124:127]\n\t"
      "v_exp_f32 v115, v115\n\t"
      "v_mfma_f32_16x16x32_bf16 v[124:127], a[124:127], v[76:79], v[124:127]\n\t"
      "ds_read_b64_tr_b16 a[78:79], v39 offset:45056\n\t"
      "v_add_f32 v30, v30, v114\n\t"
      "v_mfma_f32_16x16x32_bf16 v[192:195], a[128:131], v[96:99], v[192:195]\n\t"
      "v_exp_f32 v116, v116\n\t"
      "v_exp_f32 v117, v117\n\t"
      "v_mfma_f32_16x16x32_bf16 v[224:227], a[128:131], v[104:107], v[224:227]\n\t"
      "ds_read_b64_tr_b16 a[80:81], v40 offset:40960\n\t"
      "v_add_f32 v30, v30, v115\n\t"
      "v_cvt_pk_bf16_f32 v113, v114, v115\n\t"
      "v_mfma_f32_16x16x32_bf16 v[196:199], a[132:135], v[96:99], v[196:199]\n\t"
      "v_exp_f32 v118, v118\n\t"
      "v_exp_f32 v119, v119\n\t"
      "v_mfma_f32_16x16x32_bf16 v[228:231], a[132:135], v[104:107], v[228:231]\n\t"
      "ds_read_b64_tr_b16 a[82:83], v40 offset:45056\n\t"
      "v_add_f32 v30, v30, v116\n\t"
      "v_add_f32 v30, v30, v117\n\t"
      "v_cvt_pk_bf16_f32 v114, v116, v117\n\t"
      "v_mfma_f32_16x16x32_bf16 v[200:203], a[136:139], v[96:99], v[200:203]\n\t"
      "v_exp_f32 v120, v120\n\t"
      "v_exp_f32 v121, v121\n\t"
      "v_mfma_f32_16x16x32_bf16 v[232:235], a[136:139], v[104:107], v[232:235]\n\t"
      "ds_read_b64_tr_b16 a[84:85], v41 offset:40960\n\t"
      "v_add_f32 v30, v30, v118\n\t"
      "v_add_f32 v30, v30, v119\n\t"
      "v_cvt_pk_bf16_f32 v115, v118, v119\n\t"
      "v_mfma_f32_16x16x32_bf16 v[204:207], a[140:143], v[96:99], v[204:207]\n\t"
      "v_exp_f32 v122, v122\n\t"
      "v_exp_f32 v123, v123\n\t"
      "v_mfma_f32_16x16x32_bf16 v[236:239], a[140:143], v[104:107], v[236:239]\n\t"
      "ds_read_b64_tr_b16 a[86:87], v41 offset:45056\n\t"
      "v_add_f32 v31, v31, v120\n\t"
      "v_add_f32 v31, v31, v121\n\t"
      "v_cvt_pk_bf16_f32 v120, v120, v121\n\t"
      "s_waitcnt lgkmcnt(2)\n\t"
      "v_mfma_f32_16x16x32_bf16 v[208:211], a[144:147], v[96:99], v[208:211]\n\t"
      "v_exp_f32 v124, v124\n\t"
      "v_exp_f32 v125, v125\n\t"
      "v_mfma_f32_16x16x32_bf16 v[240:243], a[144:147], v[104:107], v[240:243]\n\t"
      "ds_read_b64_tr_b16 a[88:89], v42 offset:40960\n\t"
      "v_add_f32 v31, v31, v122\n\t"
      "v_add_f32 v31, v31, v123\n\t"
      "v_cvt_pk_bf16_f32 v121, v122, v123\n\t"
      "v_mfma_f32_16x16x32_bf16 v[212:215], a[148:151], v[96:99], v[212:215]\n\t"
      "v_exp_f32 v126, v126\n\t"
      "v_exp_f32 v127, v127\n\t"
      "v_mfma_f32_16x16x32_bf16 v[244:247], a[148:151], v[104:107], v[244:247]\n\t"
      "ds_read_b64_tr_b16 a[90:91], v42 offset:45056\n\t"
      "v_add_f32 v31, v31, v124\n\t"
      "v_add_f32 v31, v31, v125\n\t"
      "v_cvt_pk_bf16_f32 v122, v124, v125\n\t"
      "v_mfma_f32_16x16x32_bf16 v[216:219], a[152:155], v[96:99], v[216:219]\n\t"
      "v_mfma_f32_16x16x32_bf16 v[248:251], a[152:155], v[104:107], v[248:251]\n\t"
      "ds_read_b64_tr_b16 a[92:93], v43 offset:40960\n\t"
      "v_add_f32 v31, v31, v126\n\t"
      "v_add_f32 v31, v31, v127\n\t"
      "v_cvt_pk_bf16_f32 v123, v126, v127\n\t"
      "v_mfma_f32_16x16x32_bf16 v[220:223], a[156:159], v[96:99], v[220:223]\n\t"
      "v_mfma_f32_16x16x32_bf16 v[252:255], a[156:159], v[104:107], v[252:255]\n\t"
      "ds_read_b64_tr_b16 a[94:95], v43 offset:45056\n\t"
      "v_mov_b32 v20, v30\n\t"
      "v_mov_b32 v22, v31\n\t"
      "s_nop 1\n\t"
      "v_permlane16_swap_b32 v20, v30\n\t"
      "v_permlane16_swap_b32 v22, v31\n\t"
      "s_nop 1\n\t"
      "v_add_f32 v30, v30, v20\n\t"
      "v_add_f32 v31, v31, v22\n\t"
      "v_mov_b32 v20, v30\n\t"
      "v_mfma_f32_16x16x32_bf16 v[192:195], a[64:67], v[112:115], v[192:195]\n\t"
      "v_mfma_f32_16x16x32_bf16 v[224:227], a[64:67], v[120:123], v[224:227]\n\t"
      "v_mov_b32 v22, v31\n\t"
      "s_nop 1\n\t"
      "v_permlane32_swap_b32 v20, v30\n\t"
      "v_permlane32_swap_b32 v22, v31\n\t"
      "s_nop 1\n\t"
      "v_add_f32 v30, v30, v20\n\t"
      "v_add_f32 v31, v31, v22\n\t"
      "v_cmp_nlt_f32_e64 vcc, v30, s51\n\t"
      "v_cmp_nlt_f32_e64 s[42:43], v31, s51\n\t"
      "v_rcp_f32 v20, v30\n\t"
      "v_rcp_f32 v22, v31\n\t"
      "s_or_b64 s[52:53], s[52:53], vcc\n\t"
      "s_or_b64 s[52:53], s[52:53], s[42:43]\n\t"
      "v_mfma_f32_16x16x32_bf16 v[196:199], a[68:71], v[112:115], v[196:199]\n\t"
      "v_mfma_f32_16x16x32_bf16 v[228:231], a[68:71], v[120:123], v[228:231]\n\t"
      "v_mfma_f32_16x16x32_bf16 v[200:203], a[72:75], v[112:115], v[200:203]\n\t"
      "v_mfma_f32_16x16x32_bf16 v[232:235], a[72:75], v[120:123], v[232:235]\n\t"
      "v_mul_f32 v192, v192, v20\n\t"
      "v_mul_f32 v193, v193, v20\n\t"
      "v_mul_f32 v194, v194, v20\n\t"
      "v_mul_f32 v195, v195, v20\n\t"
      "v_mfma_f32_16x16x32_bf16 v[204:207], a[76:79], v[112:115], v[204:207]\n\t"
      "v_mfma_f32_16x16x32_bf16 v[236:239], a[76:79], v[120:123], v[236:239]\n\t"
      "v_mul_f32 v196, v196, v20\n\t"
      "v_mul_f32 v197, v197, v20\n\t"
      "v_mul_f32 v198, v198, v20\n\t"
      "v_mul_f32 v199, v199, v20\n\t"
      "v_cvt_pk_bf16_f32 v192, v192, v193\n\t"
      "v_cvt_pk_bf16_f32 v193, v194, v195\n\t"
      "v_mfma_f32_16x16x32_bf16 v[208:211], a[80:83], v[112:115], v[208:211]\n\t"
      "v_mfma_f32_16x16x32_bf16 v[240:243], a[80:83], v[120:123], v[240:243]\n\t"
      "v_mul_f32 v200, v200, v20\n\t"
      "v_mul_f32 v201, v201, v20\n\t"
      "v_mul_f32 v202, v202, v20\n\t"
      "v_mul_f32 v203, v203, v20\n\t"
      "v_cvt_pk_bf16_f32 v196, v196, v197\n\t"
      "v_cvt_pk_bf16_f32 v197, v198, v199\n\t"
      "s_waitcnt lgkmcnt(0)\n\t"
      "v_mfma_f32_16x16x32_bf16 v[212:215], a[84:87], v[112:115], v[212:215]\n\t"
      "v_mfma_f32_16x16x32_bf16 v[244:247], a[84:87], v[120:123], v[244:247]\n\t"
      "v_mul_f32 v204, v204, v20\n\t"
      "v_mul_f32 v205, v205, v20\n\t"
      "v_mul_f32 v206, v206, v20\n\t"
      "v_mul_f32 v207, v207, v20\n\t"
      "v_cvt_pk_bf16_f32 v200, v200, v201\n\t"
      "v_cvt_pk_bf16_f32 v201, v202, v203\n\t"
      "v_mfma_f32_16x16x32_bf16 v[216:219], a[88:91], v[112:115], v[216:219]\n\t"
      "v_mfma_f32_16x16x32_bf16 v[248:251], a[88:91], v[120:123], v[248:251]\n\t"
      "v_mul_f32 v208, v208, v20\n\t"
      "v_mul_f32 v209, v209, v20\n\t"
      "v_mul_f32 v210, v210, v20\n\t"
      "v_mul_f32 v211, v211, v20\n\t"
      "v_cvt_pk_bf16_f32 v204, v204, v205\n\t"
      "v_cvt_pk_bf16_f32 v205, v206, v207\n\t"
      "v_mfma_f32_16x16x32_bf16 v[220:223], a[92:95], v[112:115], v[220:223]\n\t"
      "v_mfma_f32_16x16x32_bf16 v[252:255], a[92:95], v[120:123], v[252:255]\n\t"
      "v_mul_f32 v212, v212, v20\n\t"
      "v_mul_f32 v213, v213, v20\n\t"
      "v_mul_f32 v214, v214, v20\n\t"
      "v_mul_f32 v215, v215, v20\n\t"
      "v_cvt_pk_bf16_f32 v208, v208, v209\n\t"
      "v_cvt_pk_bf16_f32 v209, v210, v211\n\t"
      "s_nop 1\n\t"
      "s_nop 3\n\t"
      "v_mul_f32 v216, v216, v20\n\t"
      "v_mul_f32 v217, v217, v20\n\t"
      "v_mul_f32 v218, v218, v20\n\t"
      "v_mul_f32 v219, v219, v20\n\t"
      "v_cvt_pk_bf16_f32 v212, v212, v213\n\t"
      "v_cvt_pk_bf16_f32 v213, v214, v215\n\t"
      "v_mul_f32 v220, v220, v20\n\t"
      "v_mul_f32 v221, v221, v20\n\t"
      "v_mul_f32 v222, v222, v20\n\t"
      "v_mul_f32 v223, v223, v20\n\t"
      "v_cvt_pk_bf16_f32 v216, v216, v217\n\t"
      "v_cvt_pk_bf16_f32 v217, v218, v219\n\t"
      "v_cvt_pk_bf16_f32 v220, v220, v221\n\t"
      "v_cvt_pk_bf16_f32 v221, v222, v223\n\t"
      "ds_write_b64 v8, v[192:193] offset:8192\n\t"
      "ds_write_b64 v9, v[196:197] offset:8192\n\t"
      "ds_write_b64 v10, v[200:201] offset:8192\n\t"
      "ds_write_b64 v11, v[204:205] offset:8192\n\t"
      "ds_write_b64 v12, v[208:209] offset:8192\n\t"
      "ds_write_b64 v13, v[212:213] offset:8192\n\t"
      "ds_write_b64 v14, v[216:217] offset:8192\n\t"
      "ds_write_b64 v15, v[220:221] offset:8192\n\t"
      "ds_read_b128 v[192:195], v16 offset:8192\n\t"
      "ds_read_b128 v[196:199], v17 offset:9216\n\t"
      "ds_read_b128 v[200:203], v16 offset:10240\n\t"
      "ds_read_b128 v[204:207], v17 offset:11264\n\t"
      "v_mul_f32 v224, v224, v22\n\t"
      "v_mul_f32 v225, v225, v22\n\t"
      "v_mul_f32 v226, v226, v22\n\t"
      "v_mul_f32 v227, v227, v22\n\t"
      "v_mul_f32 v228, v228, v22\n\t"
      "v_mul_f32 v229, v229, v22\n\t"
      "v_mul_f32 v230, v230, v22\n\t"
      "v_mul_f32 v231, v231, v22\n\t"
      "v_cvt_pk_bf16_f32 v224, v224, v225\n\t"
      "v_cvt_pk_bf16_f32 v225, v226, v227\n\t"
      "v_mul_f32 v232, v232, v22\n\t"
      "v_mul_f32 v233, v233, v22\n\t"
      "v_mul_f32 v234, v234, v22\n\t"
      "v_mul_f32 v235, v235, v22\n\t"
      "v_cvt_pk_bf16_f32 v228, v228, v229\n\t"
      "v_cvt_pk_bf16_f32 v229, v230, v231\n\t"
      "v_mul_f32 v236, v236, v22\n\t"
      "v_mul_f32 v237, v237, v22\n\t"
      "v_mul_f32 v238, v238, v22\n\t"
      "v_mul_f32 v239, v239, v22\n\t"
      "v_cvt_pk_bf16_f32 v232, v232, v233\n\t"
      "v_cvt_pk_bf16_f32 v233, v234, v235\n\t"
      "v_mul_f32 v240, v240, v22\n\t"
      "v_mul_f32 v241, v241, v22\n\t"
      "v_mul_f32 v242, v242, v22\n\t"
      "v_mul_f32 v243, v243, v22\n\t"
      "v_cvt_pk_bf16_f32 v236, v236, v237\n\t"
      "v_cvt_pk_bf16_f32 v237, v238, v239\n\t"
      "v_mul_f32 v244, v244, v22\n\t"
      "v_mul_f32 v245, v245, v22\n\t"
      "v_mul_f32 v246, v246, v22\n\t"
      "v_mul_f32 v247, v247, v22\n\t"
      "v_cvt_pk_bf16_f32 v240, v240, v241\n\t"
      "v_cvt_pk_bf16_f32 v241, v242, v243\n\t"
      "v_mul_f32 v248, v248, v22\n\t"
      "v_mul_f32 v249, v249, v22\n\t"
      "v_mul_f32 v250, v250, v22\n\t"
      "v_mul_f32 v251, v251, v22\n\t"
      "v_cvt_pk_bf16_f32 v244, v244, v245\n\t"
      "v_cvt_pk_bf16_f32 v245, v246, v247\n\t"
      "v_mul_f32 v252, v252, v22\n\t"
      "v_mul_f32 v253, v253, v22\n\t"
      "v_mul_f32 v254, v254, v22\n\t"
      "v_mul_f32 v255, v255, v22\n\t"
      "v_cvt_pk_bf16_f32 v248, v248, v249\n\t"
      "v_cvt_pk_bf16_f32 v249, v250, v251\n\t"
      "v_cvt_pk_bf16_f32 v252, v252, v253\n\t"
      "v_cvt_pk_bf16_f32 v253, v254, v255\n\t"
      "s_waitcnt lgkmcnt(3)\n\t"
      "s_add_u32 s45, s55, 8192\n\t"
      "buffer_store_dwordx4 v[192:195], v18, s[56:59], s45 offen offset:0 nt\n\t"
      "s_waitcnt lgkmcnt(2)\n\t"
      "buffer_store_dwordx4 v[196:199], v18, s[56:59], s45 offen offset:1024 nt\n\t"
      "s_waitcnt lgkmcnt(1)\n\t"
      "buffer_store_dwordx4 v[200:203], v18, s[56:59], s45 offen offset:2048 nt\n\t"
      "s_waitcnt lgkmcnt(0)\n\t"
      "buffer_store_dwordx4 v[204:207], v18, s[56:59], s45 offen offset:3072 nt\n\t"
      "ds_write_b64 v8, v[224:225] offset:12288\n\t"
      "ds_write_b64 v9, v[228:229] offset:12288\n\t"
      "ds_write_b64 v10, v[232:233] offset:12288\n\t"
      "ds_write_b64 v11, v[236:237] offset:12288\n\t"
      "ds_write_b64 v12, v[240:241] offset:12288\n\t"
      "ds_write_b64 v13, v[244:245] offset:12288\n\t"
      "ds_write_b64 v14, v[248:249] offset:12288\n\t"
      "ds_write_b64 v15, v[252:253] offset:12288\n\t"
      "ds_read_b128 v[224:227], v16 offset:12288\n\t"
      "ds_read_b128 v[228:231], v17 offset:13312\n\t"
      "ds_read_b128 v[232:235], v16 offset:14336\n\t"
      "ds_read_b128 v[236:239], v17 offset:15360\n\t"
      "s_waitcnt lgkmcnt(3)\n\t"
      "s_add_u32 s45, s55, 12288\n\t"
      "buffer_store_dwordx4 v[224:227], v18, s[56:59], s45 offen offset:0 nt\n\t"
      "s_waitcnt lgkmcnt(2)\n\t"
      "buffer_store_dwordx4 v[228:231], v18, s[56:59], s45 offen offset:1024 nt\n\t"
      "s_waitcnt lgkmcnt(1)\n\t"
      "buffer_store_dwordx4 v[232:235], v18, s[56:59], s45 offen offset:2048 nt\n\t"
      "s_waitcnt lgkmcnt(0)\n\t"
      "buffer_store_dwordx4 v[236:239], v18, s[56:59], s45 offen offset:3072 nt\n\t"
      "s_or_b32 s52, s52, s53\n\t"
      "s_mov_b32 %[flag], s52\n\t"
      : [flag] "=s"(flag)
      : [tid] "v"(threadIdx.x), [bh] "s"(blockIdx.x), [lds] "s"((unsigned)(size_t)lds)
        , [q] "s"(Q), [k] "s"(K), [v] "s"(V), [o] "s"(O)
      : "memory", "vcc", "scc", "s20", "s21", "s22", "s23", "s24", "s25", "s26", "s27", "s28", "s29", "s30", "s31", "s36", "s37", "s38", "s39", "s40", "s41", "s42", "s43", "s44", "s45", "s46", "s47", "s48", "s49", "s50", "s51", "s52", "s53", "s54", "s55", "s56", "s57", "s58", "s59", "s60", "s61", "s62", "s63", "s64", "s65", "s66", "s67", "s68", "s69", "s70", "s71", "v4", "v5", "v6", "v7", "v8", "v9", "v10", "v11", "v12", "v13", "v14", "v15", "v16", "v17", "v18", "v19", "v20", "v21", "v22", "v23", "v24", "v25", "v26", "v27", "v28", "v29", "v30", "v31", "v32", "v33", "v34", "v35", "v36", "v37", "v38", "v39", "v40", "v41", "v42", "v43", "v44", "v45", "v46", "v47", "v48", "v49", "v50", "v51", "v52", "v53", "v54", "v55", "v56", "v57", "v58", "v59", "v60", "v61", "v62", "v63", "v64", "v65", "v66", "v67", "v68", "v69", "v70", "v71", "v72", "v73", "v74", "v75", "v76", "v77", "v78", "v79", "v80", "v81", "v82", "v83", "v84", "v85", "v86", "v87", "v88", "v89", "v90", "v91", "v92", "v93", "v94", "v95", "v96", "v97", "v98", "v99", "v100", "v101", "v102", "v103", "v104", "v105", "v106", "v107", "v108", "v109", "v110", "v111", "v112", "v113", "v114", "v115", "v116", "v117", "v118", "v119", "v120", "v121", "v122", "v123", "v124", "v125", "v126", "v127", "v128", "v129", "v130", "v131", "v132", "v133", "v134", "v135", "v136", "v137", "v138", "v139", "v140", "v141", "v142", "v143", "v144", "v145", "v146", "v147", "v148", "v149", "v150", "v151", "v152", "v153", "v154", "v155", "v156", "v157", "v158", "v159", "v160", "v161", "v162", "v163", "v164", "v165", "v166", "v167", "v168", "v169", "v170", "v171", "v172", "v173", "v174", "v175", "v176", "v177", "v178", "v179", "v180", "v181", "v182", "v183", "v184", "v185", "v186", "v187", "v188", "v189", "v190", "v191", "v192", "v193", "v194", "v195", "v196", "v197", "v198", "v199", "v200", "v201", "v202", "v203", "v204", "v205", "v206", "v207", "v208", "v209", "v210", "v211", "v212", "v213", "v214", "v215", "v216", "v217", "v218", "v219", "v220", "v221", "v222", "v223", "v224", "v225", "v226", "v227", "v228", "v229", "v230", "v231", "v232", "v233", "v234", "v235", "v236", "v237", "v238", "v239", "v240", "v241", "v242", "v243", "v244", "v245", "v246", "v247", "v248", "v249", "v250", "v251", "v252", "v253", "v254", "v255", "a0", "a1", "a2", "a3", "a4", "a5", "a6", "a7", "a8", "a9", "a10", "a11", "a12", "a13", "a14", "a15", "a16", "a17", "a18", "a19", "a20", "a21", "a22", "a23", "a24", "a25", "a26", "a27", "a28", "a29", "a30", "a31", "a32", "a33", "a34", "a35", "a36", "a37", "a38", "a39", "a40", "a41", "a42", "a43", "a44", "a45", "a46", "a47", "a48", "a49", "a50", "a51", "a52", "a53", "a54", "a55", "a56", "a57", "a58", "a59", "a60", "a61", "a62", "a63", "a64", "a65", "a66", "a67", "a68", "a69", "a70", "a71", "a72", "a73", "a74", "a75", "a76", "a77", "a78", "a79", "a80", "a81", "a82", "a83", "a84", "a85", "a86", "a87", "a88", "a89", "a90", "a91", "a92", "a93", "a94", "a95", "a96", "a97", "a98", "a99", "a100", "a101", "a102", "a103", "a104", "a105", "a106", "a107", "a108", "a109", "a110", "a111", "a112", "a113", "a114", "a115", "a116", "a117", "a118", "a119", "a120", "a121", "a122", "a123", "a124", "a125", "a126", "a127", "a128", "a129", "a130", "a131", "a132", "a133", "a134", "a135", "a136", "a137", "a138", "a139", "a140", "a141", "a142", "a143", "a144", "a145", "a146", "a147", "a148", "a149", "a150", "a151", "a152", "a153", "a154", "a155", "a156", "a157", "a158", "a159", "a200", "a201", "a202", "a203");
  if (flag) {
    asm volatile("s_waitcnt vmcnt(0)" ::: "memory");
    const size_t bh = blockIdx.x, row = bh * 256 + threadIdx.x;
    slow_rows((const unsigned*)Q + row * 64, (const unsigned*)K + bh * 512 * 64, (const unsigned*)V + bh * 512 * 64, (unsigned*)O + row * 64);
  }
}

extern "C" void kernel_launch(void* const* d_in, const int* in_sizes, int n_in, void* d_out, int out_size, void* d_ws, size_t ws_size,
                              hipStream_t stream) {
  hipLaunchKernelGGL(attn_fwd_kernel, dim3(256), dim3(256), 0, stream, d_in[0], d_in[1], d_in[2], d_out
  );
}
```
